```python
import torch

def _softmax(x, axis=-1):
    return torch.softmax(x, dim=axis)

def _gelu(x):
    return torch.nn.functional.gelu(x, approximate='tanh')

def _silu(x):
    return torch.nn.functional.silu(x)

def _sigmoid(x):
    return torch.sigmoid(x)

def _layernorm(x, axis=-1, eps=1e-05):
    m = x.mean(dim=axis, keepdim=True)
    v = x.var(dim=axis, keepdim=True, unbiased=False)
    return (x - m) / torch.sqrt(v + eps)

def _scatter_add(tgt, idx, src, axis=0):
    return tgt.clone().index_add_(axis, idx, src)

def _segment_sum(src, seg, n):
    out = torch.zeros((n,) + src.shape[1:], dtype=src.dtype)
    return out.index_add_(0, seg, src)

def _pad(x, pw):
    flat = tuple((v for pair in reversed(pw) for v in pair))
    return torch.nn.functional.pad(x, flat)
kernel_name = 'adaLN_cond__ssm_conv_h3__dense_activation_silu__prompt'

def setup_inputs():

    _w = lambda a: (lambda t: t.to(torch.bfloat16) if t.is_floating_point() else t)(torch.as_tensor(a))

    def _A_setup():
        B, S, D = (1, 2048, 1024)
        ks = 4
        return {'q': _w((lambda *s: torch.randn(*s, dtype=torch.bfloat16) * 0.05)(B, S, D)), 'k': _w((lambda *s: torch.randn(*s, dtype=torch.bfloat16) * 0.8)(B, S, D)), 'v': _w((lambda *s: torch.randn(*s, dtype=torch.bfloat16) * 0.8)(B, S, D)), 'shift_k': _w((lambda *s: torch.randn(*s, dtype=torch.bfloat16) * 0.8)(D, ks) * 0.5), 'ssm_k': _w((lambda *s: torch.randn(*s, dtype=torch.bfloat16) * 0.8)(D, S) * (1.0 / S ** 0.5)), 'ssm_D': _w((lambda *s: torch.randn(*s, dtype=torch.bfloat16) * 0.8)(D))}
    _A_in = _A_setup()

    def _B_setup():
        B, S, D, F = (1, 2048, 1024, 3584)
        return {'x': _w((lambda *s: torch.randn(*s, dtype=torch.bfloat16) * 0.05)(B, S, D)), 'w1': _w((lambda *s: torch.randn(*s, dtype=torch.bfloat16) * 3.2)(D, F) * D ** (-0.5)), 'w2': _w((lambda *s: torch.randn(*s, dtype=torch.bfloat16) * 1.6)(F, D) * F ** (-0.5))}
    _B_in = _B_setup()
    return {'A_q': _A_in['q'], 'A_k': _A_in['k'], 'A_v': _A_in['v'], 'A_shift_k': _A_in['shift_k'], 'A_ssm_k': _A_in['ssm_k'], 'A_ssm_D': _A_in['ssm_D'], 'B_x': _B_in['x'], 'B_w1': _B_in['w1'], 'B_w2': _B_in['w2'], 'c': _w((lambda *s: torch.randn(*s, dtype=torch.bfloat16) * 0.1)(1, 1024) * 8.0), 'w_mod': _w((lambda *s: torch.randn(*s, dtype=torch.bfloat16) * 0.1)(1024, 6 * 1024) * 1024 ** (-0.5) * 8.0)}

def reference(A_q, A_k, A_v, A_shift_k, A_ssm_k, A_ssm_D, B_x, B_w1, B_w2, c, w_mod):

    def _A_ref(q, k, v, shift_k, ssm_k, ssm_D):
        B, S, D = q.shape
        ks = shift_k.shape[-1]
        k_pad = _pad(k, ((0, 0), (ks - 1, 0), (0, 0)))
        k_shift = torch.zeros_like(k)
        for t in range(ks):
            k_shift = k_shift + k_pad[:, t:t + S, :] * shift_k[:, ks - 1 - t][None, None, :]
        kv = k_shift * v
        L = 2 * S
        kv_pad = _pad(kv, ((0, 0), (0, S), (0, 0)))
        kf_pad = _pad(ssm_k, ((0, 0), (0, S)))
        kv_f = (lambda x, axes=(-2, -1): torch.fft.rfft2(x.float(), dim=axes))(kv_pad[:, None, :, :], axes=(1, 2))
        kf_f = (lambda x, axes=(-2, -1): torch.fft.rfft2(x.float(), dim=axes))(kf_pad[:, None, :], axes=(1, 2))
        kf_f = (lambda x, *d: x.permute(*d))(kf_f, 1, 2, 0)
        y_f = kv_f * kf_f[None, :, :, :]
        y = (lambda x, s, axes=(-2, -1): torch.fft.irfft2(x, s=s, dim=axes).to(torch.bfloat16))(y_f, (1, L), axes=(1, 2))[:, 0, :S, :]
        y = y + kv * ssm_D[None, None, :]
        return q * y

    def _B_ref(x, w1, w2):
        h = torch.einsum('bsd,df->bsf', x, w1)
        h = _silu(h)
        out = torch.einsum('bsf,fd->bsd', h, w2)
        return out
    x = A_q
    mod = (_silu(c) @ w_mod).reshape(c.shape[0], 6, 1, -1)
    sa, ca, ga, sb, cb, gb = (mod[:, 0].clone(), mod[:, 1].clone(), mod[:, 2].clone(), mod[:, 3].clone(), mod[:, 4].clone(), mod[:, 5].clone())
    h = _layernorm(x, axis=-1) * (1 + ca) + sa
    x = x + ga * _A_ref(h, A_k, A_v, A_shift_k, A_ssm_k, A_ssm_D)
    h = _layernorm(x, axis=-1) * (1 + cb) + sb
    x = x + gb * _B_ref(h, B_w1, B_w2)
    return x
```

```cpp
#include <hip/hip_runtime.h>
#include <hip/hip_bf16.h>

typedef unsigned u32;
typedef unsigned u32x2 __attribute__((ext_vector_type(2)));
typedef unsigned u32x4 __attribute__((ext_vector_type(4)));
typedef float f32x2 __attribute__((ext_vector_type(2)));
typedef float f32x4 __attribute__((ext_vector_type(4)));
typedef __bf16 bf16x2 __attribute__((ext_vector_type(2)));
typedef __bf16 bf16x4 __attribute__((ext_vector_type(4)));
typedef __bf16 bf16x8 __attribute__((ext_vector_type(8)));
#define LAS __attribute__((address_space(3)))
#define DI __device__ __forceinline__
typedef __amdgpu_buffer_rsrc_t rsrc_t;

constexpr int S_ = 2048, D_ = 1024, F_ = 3584;
#define AUX_IN 2
#define AUX_W 0

constexpr size_t WS_KVT = 0;
constexpr size_t WS_YB = 4u << 20;
constexpr size_t WS_X1F = 8u << 20;
constexpr size_t WS_H2F = 12u << 20;
constexpr size_t WS_HF = 16u << 20;
constexpr size_t WS_MODP = 32u << 20;
constexpr size_t WS_MODF = (32u << 20) + (256u << 10);

DI rsrc_t mkrsrc(const void* p, u32 bytes) { return __builtin_amdgcn_make_buffer_rsrc((void*)p, 0, bytes, 0x00020000); }
DI float bflo(u32 w) { return __uint_as_float(w << 16); }
DI float bfhi(u32 w) { return __uint_as_float(w & 0xffff0000u); }
DI u32 pk2(float a, float b) { f32x2 v = {a, b}; bf16x2 r = __builtin_convertvector(v, bf16x2); return __builtin_bit_cast(u32, r); }
DI float bfr(float a) { return bflo(pk2(a, 0.f)); }
DI float silu_f(float x) { return x * __builtin_amdgcn_rcpf(1.f + __builtin_amdgcn_exp2f(x * -1.44269504089f)); }
DI u32x4 ldb128(rsrc_t r, u32 off, int aux) {
  return aux == 2 ? __builtin_amdgcn_raw_buffer_load_b128(r, off, 0, 2) : __builtin_amdgcn_raw_buffer_load_b128(r, off, 0, 0);
}
DI u32x2 ldb64(rsrc_t r, u32 off, int aux) {
  return aux == 2 ? __builtin_amdgcn_raw_buffer_load_b64(r, off, 0, 2) : __builtin_amdgcn_raw_buffer_load_b64(r, off, 0, 0);
}
DI bf16x4 tr_read(u32 a) { return __builtin_amdgcn_ds_read_tr16_b64_v4bf16((LAS bf16x4*)(size_t)a); }
DI bf16x8 cat8(bf16x4 a, bf16x4 b) { return __builtin_shufflevector(a, b, 0, 1, 2, 3, 4, 5, 6, 7); }
template <class T> DI T lds_ld(u32 a) { return *(LAS T*)(size_t)a; }
template <class T> DI void lds_st(u32 a, T v) { *(LAS T*)(size_t)a = v; }
DI void unpack8(u32x4 w, float* f) {
#pragma unroll
  for (int i = 0; i < 4; ++i) { u32 x = w[i]; f[2 * i] = bflo(x); f[2 * i + 1] = bfhi(x); }
}

__global__ __launch_bounds__(256) void k1_prep(const void* __restrict__ k, const void* __restrict__ v, const void* __restrict__ shk,
                                               const void* __restrict__ c, const void* __restrict__ wmod, char* __restrict__ ws) {
  __shared__ __attribute__((aligned(16))) char smem[64 * 320];
  const u32 sb = (u32)(size_t)(LAS char*)smem;
  const int t = threadIdx.x, b = blockIdx.x;
  if (b < 256) {
    const int dblk = b & 7, sblk = b >> 3;
    const int a = t >> 4, cc = t & 15;
    const int s0 = sblk * 64 + a * 4, ch0 = dblk * 128 + cc * 8;
    rsrc_t rk = mkrsrc(k, S_ * D_ * 2), rv = mkrsrc(v, S_ * D_ * 2), rs = mkrsrc(shk, D_ * 4 * 2);
    u32x4 kk[7], vv[4], sh[4];
#pragma unroll
    for (int j = 0; j < 7; ++j) kk[j] = ldb128(rk, (u32)((s0 - 3 + j) * 2048 + ch0 * 2), AUX_IN);
#pragma unroll
    for (int j = 0; j < 4; ++j) vv[j] = ldb128(rv, (u32)((s0 + j) * 2048 + ch0 * 2), AUX_IN);
#pragma unroll
    for (int j = 0; j < 4; ++j) sh[j] = ldb128(rs, (u32)(ch0 * 8 + j * 16), 0);
    float shf[8][4];
#pragma unroll
    for (int j = 0; j < 4; ++j) { float f[8]; unpack8(sh[j], f);
#pragma unroll
      for (int i = 0; i < 8; ++i) shf[2 * j + (i >> 2)][i & 3] = f[i]; }
    float kf[7][8];
#pragma unroll
    for (int j = 0; j < 7; ++j) unpack8(kk[j], kf[j]);
#pragma unroll
    for (int r = 0; r < 4; ++r) {
      float vf[8], o[8]; unpack8(vv[r], vf);
#pragma unroll
      for (int e = 0; e < 8; ++e) {
        float x = kf[r][e] * shf[e][3];
        x = fmaf(kf[r + 1][e], shf[e][2], x);
        x = fmaf(kf[r + 2][e], shf[e][1], x);
        x = fmaf(kf[r + 3][e], shf[e][0], x);
        o[e] = x * vf[e];
      }
      u32x4 w = {pk2(o[0], o[1]), pk2(o[2], o[3]), pk2(o[4], o[5]), pk2(o[6], o[7])};
      lds_st<u32x4>(sb + (a * 4 + r) * 320 + cc * 16, w);
    }
    __syncthreads();
    const int w = t >> 6, l = t & 63, g = l >> 4, q = (l & 15) >> 2, p = l & 3;
    u32x4* dst = (u32x4*)(ws + WS_KVT);
#pragma unroll
    for (int i = 0; i < 4; ++i) {
      const int u = w * 4 + i, s8 = u >> 1, hf = u & 1;
      const u32 ad = sb + (s8 * 8 + q) * 320 + (hf * 64 + g * 16 + p * 4) * 2;
      bf16x8 r = cat8(tr_read(ad), tr_read(ad + 4 * 320));
      dst[(size_t)(sblk * 8 + s8) * 1024 + dblk * 128 + hf * 64 + l] = __builtin_bit_cast(u32x4, r);
    }
  } else {
    const int bb = b - 256, ksl = bb & 7, nb = bb >> 3;
    const int cc = t & 31, rg = t >> 5;
    const int r0 = ksl * 128 + rg * 16;
    rsrc_t rw = mkrsrc(wmod, 1024 * 6144 * 2), rc = mkrsrc(c, 1024 * 2);
    u32x4 wv[16];
#pragma unroll
    for (int j = 0; j < 16; ++j) wv[j] = ldb128(rw, (u32)((r0 + j) * 12288 + nb * 512 + cc * 16), AUX_IN);
    u32x4 c0 = ldb128(rc, r0 * 2, 0), c1 = ldb128(rc, r0 * 2 + 16, 0);
    float cf[16]; unpack8(c0, cf); unpack8(c1, cf + 8);
    float acc[8] = {0, 0, 0, 0, 0, 0, 0, 0};
#pragma unroll
    for (int j = 0; j < 16; ++j) {
      const float sc = bfr(silu_f(cf[j]));
      float f[8]; unpack8(wv[j], f);
#pragma unroll
      for (int e = 0; e < 8; ++e) acc[e] = fmaf(sc, f[e], acc[e]);
    }
    lds_st<f32x4>(sb + (rg * 256 + cc * 8) * 4, f32x4{acc[0], acc[1], acc[2], acc[3]});
    lds_st<f32x4>(sb + (rg * 256 + cc * 8) * 4 + 16, f32x4{acc[4], acc[5], acc[6], acc[7]});
    __syncthreads();
    float sum = 0.f;
#pragma unroll
    for (int j = 0; j < 8; ++j) sum += lds_ld<float>(sb + (j * 256 + t) * 4);
    ((float*)(ws + WS_MODP))[ksl * 6144 + nb * 256 + t] = sum;
  }
}

constexpr int U_STR = 4576, C_STR = 4160, C_CH = 4 * C_STR, C_BASE = 4 * U_STR;
constexpr int K2_LDS = C_BASE + 4 * C_CH;
__global__ __launch_bounds__(256) void k2_conv(const void* __restrict__ ssmk, const void* __restrict__ ssmd, char* __restrict__ ws) {
  extern __shared__ __attribute__((aligned(16))) char dsm[];
  const u32 sb = (u32)(size_t)(LAS char*)dsm;
  const int t = threadIdx.x, b = blockIdx.x;
  const int cg = (b & 7) * 32 + (b >> 3);
  const int w = __builtin_amdgcn_readfirstlane(t >> 6), l = t & 63;
  const int ch = cg * 4 + w;
  rsrc_t rk = mkrsrc(ssmk, D_ * S_ * 2), ru = mkrsrc(ws + WS_KVT, S_ * D_ * 2);
  u32x4 kw[4], uw[4]; u32x2 kh[4];
#pragma unroll
  for (int q = 0; q < 4; ++q) {
    const u32 o = (u32)(ch * 4096 + (l + 64 * q) * 16);
    kw[q] = ldb128(rk, o, AUX_IN);
    kh[q] = ldb64(rk, o - 8, AUX_IN);
  }
#pragma unroll
  for (int q = 0; q < 4; ++q) uw[q] = ldb128(ru, (u32)((((t >> 2) + 64 * q) * 1024 + cg * 4 + (t & 3)) * 16), 0);
  const float dval = bflo(((const unsigned short*)ssmd)[ch]);
  if (t < 24) {
    const int n = b * 24 + t; float sum = 0.f;
    const float* mp = (const float*)(ws + WS_MODP);
#pragma unroll
    for (int j = 0; j < 8; ++j) sum += mp[j * 6144 + n];
    sum = bfr(sum);
    const int which = n >> 10;
    if (which == 1 || which == 4) sum = bfr(1.f + sum);
    ((float*)(ws + WS_MODF))[n] = sum;
  }
  const u32x4 z4 = {0, 0, 0, 0};
  if (l < 30) lds_st<u32x4>(sb + w * U_STR + l * 16, z4);
  if (l < 8) lds_st<u32x4>(sb + C_BASE + w * C_CH + (l >> 1) * C_STR + 4096 + (l & 1) * 16, z4);
  if (l == 0) kh[0] = u32x2{0, 0};
#pragma unroll
  for (int q = 0; q < 4; ++q) {
    const u32 wm2 = kh[q][0], wm1 = kh[q][1], w0 = kw[q][0], w1 = kw[q][1], w2 = kw[q][2], w3 = kw[q][3];
    const u32 sm1 = __builtin_amdgcn_perm(wm1, wm1, 0x01000302u), s0 = __builtin_amdgcn_perm(w0, w0, 0x01000302u), s1 = __builtin_amdgcn_perm(w1, w1, 0x01000302u),
              s2 = __builtin_amdgcn_perm(w2, w2, 0x01000302u), s3 = __builtin_amdgcn_perm(w3, w3, 0x01000302u);
    const u32 cm1 = __builtin_amdgcn_perm(wm1, wm2, 0x03020504u), c0 = __builtin_amdgcn_perm(w0, wm1, 0x03020504u), c1 = __builtin_amdgcn_perm(w1, w0, 0x03020504u),
              c2 = __builtin_amdgcn_perm(w2, w1, 0x03020504u), c3 = __builtin_amdgcn_perm(w3, w2, 0x03020504u);
    const u32 ad = sb + C_BASE + w * C_CH + (255 - (l + 64 * q)) * 16;
    lds_st<u32x4>(ad + 0 * C_STR, u32x4{c2, c1, c0, cm1});
    lds_st<u32x4>(ad + 1 * C_STR, u32x4{s2, s1, s0, sm1});
    lds_st<u32x4>(ad + 2 * C_STR, u32x4{c3, c2, c1, c0});
    lds_st<u32x4>(ad + 3 * C_STR, u32x4{s3, s2, s1, s0});
  }
#pragma unroll
  for (int q = 0; q < 4; ++q) lds_st<u32x4>(sb + (t & 3) * U_STR + 480 + ((t >> 2) + 64 * q) * 16, uw[q]);
  __syncthreads();

  const int r = l & 15, g = l >> 4;
  const u32 aA = sb + C_BASE + w * C_CH + (r & 3) * C_STR + 24 - 8 * (r >> 2) + 16 * g;
  const u32 aB = sb + w * U_STR + 32 * r + 16 * g;
  f32x4 acc[8];
#pragma unroll
  for (int i = 0; i < 8; ++i) acc[i] = f32x4{0, 0, 0, 0};
  bf16x8 A[2][8], B[2][8];
#define LOADF(X, T0)                                                                                            \
  _Pragma("unroll") for (int i = 0; i < 8; ++i) {                                                               \
    B[X][i] = lds_ld<bf16x8>(aB + 512 * i + 64 * (7 - (T0)));                                                   \
    A[X][i] = cat8(lds_ld<bf16x4>(aA + 64 * (63 - 8 * i - (T0))), lds_ld<bf16x4>(aA + 64 * (63 - 8 * i - (T0)) + 8)); \
  }
#define MMAF(X)                                                                                                 \
  _Pragma("unroll") for (int n = 0; n < 8; ++n)                                                                 \
  _Pragma("unroll") for (int i = 0; i <= n; ++i)                                                                \
    acc[n] = __builtin_amdgcn_mfma_f32_16x16x32_bf16(A[X][i], B[X][n - i], acc[n], 0, 0, 0);
  LOADF(0, 0)
#pragma unroll
  for (int t0 = 0; t0 < 8; t0 += 2) {
    LOADF(1, t0 + 1)
    MMAF(0)
    if (t0 + 2 < 8) { LOADF(0, t0 + 2) }
    MMAF(1)
  }
#undef LOADF
#undef MMAF
#pragma unroll
  for (int n = 0; n < 8; ++n) {
    const u32 ad = sb + w * U_STR + 480 + 512 * n + 32 * r + 8 * g;
    const u32x2 u = lds_ld<u32x2>(ad);
    const u32 u0 = u[0], u1 = u[1];
    const u32 y01 = pk2(acc[n][0], acc[n][1]), y23 = pk2(acc[n][2], acc[n][3]);
    const u32 d01 = pk2(bflo(u0) * dval, bfhi(u0) * dval), d23 = pk2(bflo(u1) * dval, bfhi(u1) * dval);
    lds_st<u32x2>(ad, u32x2{pk2(bflo(y01) + bflo(d01), bfhi(y01) + bfhi(d01)), pk2(bflo(y23) + bflo(d23), bfhi(y23) + bfhi(d23))});
  }
  __syncthreads();
  {
    const int q = (l & 15) >> 2, p = l & 3;
    u32x2* dst = (u32x2*)(ws + WS_YB) + (size_t)cg * 2048;
#pragma unroll
    for (int j = 0; j < 8; ++j) {
      const int s0 = w * 512 + j * 64;
      bf16x4 y = tr_read(sb + q * U_STR + 480 + (s0 + g * 16 + p * 4) * 2);
      dst[s0 + l] = __builtin_bit_cast(u32x2, y);
    }
  }
}

DI float red8(float x) {
  x += __shfl_xor(x, 8); x += __shfl_xor(x, 16); x += __shfl_xor(x, 32); return x;
}
DI void row_stats(const float (&x)[16], u32 slot, int w, int l, float& mean, float& rstd) {
  float s = 0.f;
#pragma unroll
  for (int e = 0; e < 16; ++e) s += x[e];
  const float mw = red8(s) * (1.f / 128.f);
  float qv = 0.f;
#pragma unroll
  for (int e = 0; e < 16; ++e) { const float d = x[e] - mw; qv = fmaf(d, d, qv); }
  qv = red8(qv);
  if (l < 8) lds_st<f32x2>(slot + (l * 8 + w) * 8, f32x2{mw, qv});
  __syncthreads();
  f32x4 p[4];
#pragma unroll
  for (int i = 0; i < 4; ++i) p[i] = lds_ld<f32x4>(slot + (l & 7) * 64 + i * 16);
  float m = 0.f;
#pragma unroll
  for (int i = 0; i < 4; ++i) m += p[i][0] + p[i][2];
  m *= 0.125f;
  float m2 = 0.f, dd = 0.f;
#pragma unroll
  for (int i = 0; i < 4; ++i) { m2 += p[i][1] + p[i][3]; const float d0 = p[i][0] - m, d1 = p[i][2] - m; dd = fmaf(d0, d0, dd); dd = fmaf(d1, d1, dd); }
  mean = m;
  rstd = __builtin_amdgcn_rsqf(fmaf(dd, 128.f, m2) * (1.f / 1024.f) + 1e-5f);
}
__global__ __launch_bounds__(512) void k3_norm(const void* __restrict__ xin, char* __restrict__ ws) {
  __shared__ __attribute__((aligned(16))) char smem[1024];
  const u32 sb = (u32)(size_t)(LAS char*)smem;
  const int t = threadIdx.x, b = blockIdx.x;
  const int rb = (b & 7) * 32 + (b >> 3);
  const int w = t >> 6, l = t & 63, row = l & 7, cc = l >> 3;
  const int s = rb * 8 + row;
  rsrc_t rx = mkrsrc(xin, S_ * D_ * 2), ry = mkrsrc(ws + WS_YB, S_ * D_ * 2), rm = mkrsrc(ws + WS_MODF, 6144 * 4);
  u32x4 xw[2]; u32x2 yw[2][2]; f32x4 md[5][2][2];
#pragma unroll
  for (int j = 0; j < 2; ++j) {
    const int cj = w * 16 + j * 8 + cc;
    xw[j] = ldb128(rx, (u32)(s * 2048 + cj * 16), AUX_IN);
    yw[j][0] = ldb64(ry, (u32)(((2 * cj) * 2048 + s) * 8), 0);
    yw[j][1] = ldb64(ry, (u32)(((2 * cj + 1) * 2048 + s) * 8), 0);
  }
#pragma unroll
  for (int i = 0; i < 5; ++i)
#pragma unroll
    for (int j = 0; j < 2; ++j)
#pragma unroll
      for (int h = 0; h < 2; ++h) md[i][j][h] = __builtin_bit_cast(f32x4, ldb128(rm, (u32)(i * 4096 + (w * 16 + j * 8 + cc) * 32 + h * 16), 0));
  float x[16], y[16];
  unpack8(xw[0], x); unpack8(xw[1], x + 8);
#pragma unroll
  for (int j = 0; j < 2; ++j) unpack8(u32x4{yw[j][0][0], yw[j][0][1], yw[j][1][0], yw[j][1][1]}, y + 8 * j);
  float mean, rstd;
  row_stats(x, sb, w, l, mean, rstd);
  u32 x1p[8];
  {
    const float nm = -mean * rstd; float x1[16];
#pragma unroll
    for (int e = 0; e < 16; ++e) {
      const float sa = md[0][e >> 3][(e >> 2) & 1][e & 3], ca = md[1][e >> 3][(e >> 2) & 1][e & 3], ga = md[2][e >> 3][(e >> 2) & 1][e & 3];
      const float h = fmaf(fmaf(x[e], rstd, nm), ca, sa);
      x1[e] = fmaf(ga, h * y[e], x[e]);
    }
#pragma unroll
    for (int e = 0; e < 8; ++e) { x1p[e] = pk2(x1[2 * e], x1[2 * e + 1]); x[2 * e] = bflo(x1p[e]); x[2 * e + 1] = bfhi(x1p[e]); }
  }
  const size_t fo = (size_t)(s >> 7) * 32 * 8192 + ((s >> 4) & 7) * 1024 + (s & 15) * 16;
#pragma unroll
  for (int j = 0; j < 2; ++j) {
    const int cj = w * 16 + j * 8 + cc;
    *(u32x4*)(ws + WS_X1F + fo + (cj >> 2) * 8192 + (cj & 3) * 256) = u32x4{x1p[4 * j], x1p[4 * j + 1], x1p[4 * j + 2], x1p[4 * j + 3]};
  }
  row_stats(x, sb + 512, w, l, mean, rstd);
  {
    const float nm = -mean * rstd; float h[16];
#pragma unroll
    for (int e = 0; e < 16; ++e) {
      const float sbv = md[3][e >> 3][(e >> 2) & 1][e & 3], cb = md[4][e >> 3][(e >> 2) & 1][e & 3];
      h[e] = fmaf(fmaf(x[e], rstd, nm), cb, sbv);
    }
#pragma unroll
    for (int j = 0; j < 2; ++j) {
      const int cj = w * 16 + j * 8 + cc;
      *(u32x4*)(ws + WS_H2F + fo + (cj >> 2) * 8192 + (cj & 3) * 256) =
          u32x4{pk2(h[8 * j], h[8 * j + 1]), pk2(h[8 * j + 2], h[8 * j + 3]), pk2(h[8 * j + 4], h[8 * j + 5]), pk2(h[8 * j + 6], h[8 * j + 7])};
    }
  }
}

template <int N> DI void wait_vm() { asm volatile("s_waitcnt vmcnt(%0)" ::"n"(N) : "memory"); }
DI void wg_bar() { asm volatile("s_barrier" ::: "memory"); }
DI void dma16(u32 voff, rsrc_t r, u32 soff, u32 ldsaddr, int aux) {
  if (aux == 2) asm volatile("s_mov_b32 m0, %2\n\ts_nop 0\n\tbuffer_load_dwordx4 %0, %1, %3 offen nt lds" ::"v"(voff), "s"(r), "s"(ldsaddr), "s"(soff) : "memory");
  else asm volatile("s_mov_b32 m0, %2\n\ts_nop 0\n\tbuffer_load_dwordx4 %0, %1, %3 offen lds" ::"v"(voff), "s"(r), "s"(ldsaddr), "s"(soff) : "memory");
}

template <int MODE, int NT_, int KS_, int NS_, int SY_, int LDB_>
struct GC {
  static constexpr int NT = NT_, KS = KS_, NS = NS_, SY = SY_, LDB = LDB_;
  static constexpr int BN = 32 * NT, RS = BN * 2, BST = 32 * RS, SB = 8192 + BST, PB = BST / 1024, NB = KS / SY;
  static constexpr int LDS = NS * SB;
  static constexpr int mn(int a, int b) { return a < b ? a : b; }
  static constexpr int need(int j) { return mn(SY * (j + 1), KS - 1); }
  static constexpr int issued(int j) { return j <= 0 ? mn(NS - 1, KS - 1) : mn(SY * (j - 1) + NS, KS - 1); }
  static DI int swz(int row) { return MODE == 1 ? ((row >> 3) & 1) : (RS == 128 ? ((row >> 1) & 3) : (row & 7)); }
};

template <class C, int NPB> struct Loader {
  u32 voffA, voffB[NPB]; rsrc_t rA, rB; u32 soffA, soffB, ldsA, ldsB;
  template <int ST> DI void stage() {
    constexpr int slot = ST % C::NS;
#pragma unroll
    for (int i = 0; i < 2; ++i) dma16(voffA, rA, soffA + ST * 8192 + i * 1024, ldsA + slot * C::SB + i * 1024, 0);
#pragma unroll
    for (int i = 0; i < NPB; ++i) dma16(voffB[i], rB, soffB + ST * 32 * C::LDB, ldsB + slot * C::SB + i * 1024, AUX_W);
  }
  template <int A, int B> DI void stages() { if constexpr (A <= B) { stage<A>(); stages<A + 1, B>(); } }
  template <int J> DI void step() {
    if constexpr (J < C::NB) {
      wait_vm<(C::issued(J) - C::need(J)) * (2 + NPB)>();
      wg_bar();
      if constexpr (J >= 0) stages<C::issued(J) + 1, C::issued(J + 1)>();
      step<J + 1>();
    }
  }
  DI void run(int lw, int pb0, int l, u32 sb, const void* Aptr, u32 Abytes, u32 aoff, const void* Bptr, u32 Bbytes, u32 boff) {
    rA = mkrsrc(Aptr, Abytes); rB = mkrsrc(Bptr, Bbytes);
    voffA = l * 16; soffA = aoff + lw * 2048; ldsA = sb + lw * 2048;
    soffB = boff; ldsB = sb + 8192 + pb0 * 1024;
#pragma unroll
    for (int i = 0; i < NPB; ++i) {
      const int o = (pb0 + i) * 1024 + l * 16, row = o / C::RS, cb = o % C::RS;
      voffB[i] = row * C::LDB + ((((cb >> 5) ^ C::swz(row)) << 5) | (cb & 31));
    }
    stages<0, C::issued(0)>();
    step<-1>();
  }
};

template <class C, int MODE> struct Consumer {
  static constexpr int NBASE = MODE == 1 ? 2 : C::NT;
  f32x4 acc[4][C::NT];
  bf16x8 Af[2][4], Bf[2][C::NT];
  u32 a0, b0[NBASE];
  template <int X> DI void rd(u32 so) {
    const u32 a = a0 + so;
    u32 bb[NBASE];
#pragma unroll
    for (int i = 0; i < NBASE; ++i) bb[i] = b0[i] + so;
    constexpr int TOFF = (MODE == 1 ? 4 : 16) * C::RS;
#pragma unroll
    for (int j = 0; j < C::NT; ++j) {
      const u32 ad = MODE == 1 ? bb[j & 1] + 32 * j : bb[j % NBASE];
      Bf[X][j] = cat8(tr_read(ad), tr_read(ad + TOFF));
      if (j < 4) Af[X][j] = lds_ld<bf16x8>(a + j * 1024);
    }
#pragma unroll
    for (int j = C::NT; j < 4; ++j) Af[X][j] = lds_ld<bf16x8>(a + j * 1024);
  }
  template <int X> DI void mma() {
#pragma unroll
    for (int j = 0; j < C::NT; ++j)
#pragma unroll
      for (int m = 0; m < 4; ++m) acc[m][j] = __builtin_amdgcn_mfma_f32_16x16x32_bf16(Bf[X][j], Af[X][m], acc[m][j], 0, 0, 0);
  }
  DI void sched() {
    constexpr int NR = 4 + 2 * C::NT, NM = 4 * C::NT;
    constexpr int RPM = (NR + NM - 1) / NM;
#pragma unroll
    for (int i = 0; i < NM; ++i) {
      __builtin_amdgcn_sched_group_barrier(0x008, 1, 0);
      __builtin_amdgcn_sched_group_barrier(0x100, RPM, 0);
    }
  }
  DI void sync() {
    asm volatile("s_waitcnt lgkmcnt(0)" ::: "memory");
    __builtin_amdgcn_sched_barrier(0);
    wg_bar();
    __builtin_amdgcn_sched_barrier(0);
  }
  DI void run(int wm, int wc, int l, u32 sb) {
    const int g = l >> 4, q = (l & 15) >> 2, p = l & 3;
    a0 = sb + wm * 4096 + l * 16;
    if constexpr (MODE == 1) {
      const int base = sb + 8192 + (8 * g + q) * C::RS + 8 * p + 32 * C::NT * wc;
      const int dl = (g & 1) * 32;
      b0[0] = base + ((wc & C::NT & 1) ? -dl : dl);
      b0[1] = base + ((wc & C::NT & 1) ? dl : -dl);
    } else {
      const int row = 4 * g + q;
#pragma unroll
      for (int j = 0; j < C::NT; ++j) b0[j] = sb + 8192 + row * C::RS + 8 * p + 32 * ((C::NT * wc + j) ^ C::swz(row));
    }
#pragma unroll
    for (int m = 0; m < 4; ++m)
#pragma unroll
      for (int j = 0; j < C::NT; ++j) acc[m][j] = f32x4{0, 0, 0, 0};
    wg_bar();
    __builtin_amdgcn_sched_barrier(0);
    rd<0>(0);
    u32 so = C::SB;
    static_assert(C::SY % 2 == 0 && C::KS % C::SY == 0, "steps");
#pragma unroll 1
    for (int j = 0; j < C::NB - 1; ++j) {
      sync();
#pragma unroll
      for (int u = 0; u < C::SY; u += 2) {
        rd<1>(so); mma<0>(); sched();
        so += C::SB; if (so == C::LDS) so = 0;
        __builtin_amdgcn_sched_barrier(0);
        rd<0>(so); mma<1>(); sched();
        so += C::SB; if (so == C::LDS) so = 0;
        __builtin_amdgcn_sched_barrier(0);
      }
    }
    sync();
#pragma unroll
    for (int u = 0; u < C::SY; u += 2) {
      rd<1>(so); mma<0>(); sched();
      so += C::SB; if (so == C::LDS) so = 0;
      __builtin_amdgcn_sched_barrier(0);
      if (u + 2 < C::SY) { rd<0>(so); mma<1>(); sched(); so += C::SB; if (so == C::LDS) so = 0; }
      else mma<1>();
      __builtin_amdgcn_sched_barrier(0);
    }
  }
};

typedef GC<1, 7, 32, 7, 2, F_ * 2> G1;
__global__ __launch_bounds__(512, 2) void k4_up(const void* __restrict__ w1, char* __restrict__ ws) {
  extern __shared__ __attribute__((aligned(16))) char dsm[];
  const u32 sb = (u32)(size_t)(LAS char*)dsm;
  const int t = threadIdx.x, b = blockIdx.x, w = __builtin_amdgcn_readfirstlane(t >> 6), l = t & 63;
  const int x = b & 7, idx = b >> 3;
  const int pm = (x & 1) * 8 + (idx & 7), pn = (x >> 1) * 4 + (idx >> 3);
  if (w >= 4) {
    const int lw = w - 4;
    if (lw < 2) { Loader<G1, 4> L; L.run(lw, lw * 4, l, sb, ws + WS_H2F, S_ * D_ * 2, pm * 32 * 8192, w1, D_ * F_ * 2, pn * 448); }
    else        { Loader<G1, 3> L; L.run(lw, 8 + (lw - 2) * 3, l, sb, ws + WS_H2F, S_ * D_ * 2, pm * 32 * 8192, w1, D_ * F_ * 2, pn * 448); }
    return;
  }
  const int wm = w >> 1, wc = w & 1;
  Consumer<G1, 1> c;
  c.run(wm, wc, l, sb);
  char* hb = ws + WS_HF + ((size_t)(pm * 112 + pn * 7) * 8 + wm * 4) * 1024 + l * 16;
#pragma unroll
  for (int m = 0; m < 4; ++m) {
    u32x2 o[7];
#pragma unroll
    for (int j = 0; j < 7; ++j) { const f32x4 a = c.acc[m][j]; o[j] = u32x2{pk2(silu_f(a[0]), silu_f(a[1])), pk2(silu_f(a[2]), silu_f(a[3]))}; }
    if (wc == 0) {
#pragma unroll
      for (int j = 0; j < 3; ++j) *(u32x4*)(hb + j * 8192 + m * 1024) = u32x4{o[2 * j][0], o[2 * j][1], o[2 * j + 1][0], o[2 * j + 1][1]};
      *(u32x2*)(hb + 3 * 8192 + m * 1024) = o[6];
    } else {
      *(u32x2*)(hb + 3 * 8192 + m * 1024 + 8) = o[0];
#pragma unroll
      for (int j = 0; j < 3; ++j) *(u32x4*)(hb + (4 + j) * 8192 + m * 1024) = u32x4{o[2 * j + 1][0], o[2 * j + 1][1], o[2 * j + 2][0], o[2 * j + 2][1]};
    }
  }
}

typedef GC<2, 2, 112, 12, 4, D_ * 2> G2;
__global__ __launch_bounds__(512, 2) void k5_down(const void* __restrict__ w2, char* __restrict__ ws, unsigned short* __restrict__ out) {
  extern __shared__ __attribute__((aligned(16))) char dsm[];
  const u32 sb = (u32)(size_t)(LAS char*)dsm;
  const int t = threadIdx.x, b = blockIdx.x, w = __builtin_amdgcn_readfirstlane(t >> 6), l = t & 63;
  const int x = b & 7, idx = b >> 3;
  const int pm = (x & 3) * 4 + (idx & 3), pn = (x >> 2) * 8 + (idx >> 2);
  if (w >= 4) {
    const int lw = w - 4;
    Loader<G2, 1> L; L.run(lw, lw, l, sb, ws + WS_HF, S_ * F_ * 2, pm * 112 * 8192, w2, F_ * D_ * 2, pn * 128);
    return;
  }
  const int wm = w >> 1, wc = w & 1, g = l >> 4;
  f32x4 gb[2]; u32x2 xr[4][2];
#pragma unroll
  for (int j = 0; j < 2; ++j) {
    const int jt = pn * 4 + wc * 2 + j;
    gb[j] = *(const f32x4*)(ws + WS_MODF + (5 * 1024 + jt * 16 + 4 * g) * 4);
#pragma unroll
    for (int m = 0; m < 4; ++m)
      xr[m][j] = *(const u32x2*)(ws + WS_X1F + ((size_t)(pm * 32 + (jt >> 1)) * 8 + wm * 4 + m) * 1024 + ((2 * (jt & 1) + (g >> 1)) * 16 + (l & 15)) * 16 + (g & 1) * 8);
  }
  Consumer<G2, 2> c;
  c.run(wm, wc, l, sb);
#pragma unroll
  for (int m = 0; m < 4; ++m)
#pragma unroll
    for (int j = 0; j < 2; ++j) {
      const f32x4 a = c.acc[m][j]; const u32 x0 = xr[m][j][0], x1 = xr[m][j][1];
      const u32 o01 = pk2(a[0], a[1]), o23 = pk2(a[2], a[3]);
      const u32x2 r = {pk2(fmaf(gb[j][0], bflo(o01), bflo(x0)), fmaf(gb[j][1], bfhi(o01), bfhi(x0))),
                       pk2(fmaf(gb[j][2], bflo(o23), bflo(x1)), fmaf(gb[j][3], bfhi(o23), bfhi(x1)))};
      *(u32x2*)(out + (size_t)(pm * 128 + wm * 64 + m * 16 + (l & 15)) * 1024 + (pn * 4 + wc * 2 + j) * 16 + 4 * g) = r;
    }
}

extern "C" void kernel_launch(void* const* d_in, const int* in_sizes, int n_in, void* d_out, int out_size, void* d_ws, size_t ws_size,
                              hipStream_t stream) {
  char* ws = (char*)d_ws;
  (void)hipFuncSetAttribute((const void*)k2_conv, hipFuncAttributeMaxDynamicSharedMemorySize, K2_LDS);
  (void)hipFuncSetAttribute((const void*)k4_up, hipFuncAttributeMaxDynamicSharedMemorySize, G1::LDS);
  (void)hipFuncSetAttribute((const void*)k5_down, hipFuncAttributeMaxDynamicSharedMemorySize, G2::LDS);
  k1_prep<<<448, 256, 0, stream>>>(d_in[1], d_in[2], d_in[3], d_in[9], d_in[10], ws);
  k2_conv<<<256, 256, K2_LDS, stream>>>(d_in[4], d_in[5], ws);
  k3_norm<<<256, 512, 0, stream>>>(d_in[0], ws);
  k4_up<<<256, 512, G1::LDS, stream>>>(d_in[7], ws);
  k5_down<<<256, 512, G2::LDS, stream>>>(d_in[8], ws, (unsigned short*)d_out);
}
```

```cpp
#define PREAUX 0
#include <hip/hip_runtime.h>
#include <hip/hip_bf16.h>

typedef unsigned u32;
typedef unsigned u32x2 __attribute__((ext_vector_type(2)));
typedef unsigned u32x4 __attribute__((ext_vector_type(4)));
typedef float f32x2 __attribute__((ext_vector_type(2)));
typedef float f32x4 __attribute__((ext_vector_type(4)));
typedef __bf16 bf16x2 __attribute__((ext_vector_type(2)));
typedef __bf16 bf16x4 __attribute__((ext_vector_type(4)));
typedef __bf16 bf16x8 __attribute__((ext_vector_type(8)));
#define LAS __attribute__((address_space(3)))
#define DI __device__ __forceinline__
typedef __amdgpu_buffer_rsrc_t rsrc_t;

constexpr int S_ = 2048, D_ = 1024, F_ = 3584;
#define AUX_IN 0
#define AUX_W 0

constexpr size_t WS_KVT = 0;
constexpr size_t WS_YB = 4u << 20;
constexpr size_t WS_X1F = 8u << 20;
constexpr size_t WS_H2F = 12u << 20;
constexpr size_t WS_HF = 16u << 20;
constexpr size_t WS_MODP = 32u << 20;
constexpr size_t WS_MODF = (32u << 20) + (256u << 10);

#define STAMP(kid, slot)
DI rsrc_t mkrsrc(const void* p, u32 bytes) { return __builtin_amdgcn_make_buffer_rsrc((void*)p, 0, bytes, 0x00020000); }
DI float bflo(u32 w) { return __uint_as_float(w << 16); }
DI float bfhi(u32 w) { return __uint_as_float(w & 0xffff0000u); }
DI u32 pk2(float a, float b) { f32x2 v = {a, b}; bf16x2 r = __builtin_convertvector(v, bf16x2); return __builtin_bit_cast(u32, r); }
DI float bfr(float a) { return bflo(pk2(a, 0.f)); }
DI float silu_f(float x) { return x * __builtin_amdgcn_rcpf(1.f + __builtin_amdgcn_exp2f(x * -1.44269504089f)); }
DI u32x4 ldb128(rsrc_t r, u32 off, int aux) {
  return aux == 2 ? __builtin_amdgcn_raw_buffer_load_b128(r, off, 0, 2) : __builtin_amdgcn_raw_buffer_load_b128(r, off, 0, 0);
}
DI u32x2 ldb64(rsrc_t r, u32 off, int aux) {
  return aux == 2 ? __builtin_amdgcn_raw_buffer_load_b64(r, off, 0, 2) : __builtin_amdgcn_raw_buffer_load_b64(r, off, 0, 0);
}
DI bf16x4 tr_read(u32 a) { return __builtin_amdgcn_ds_read_tr16_b64_v4bf16((LAS bf16x4*)(size_t)a); }
DI bf16x8 cat8(bf16x4 a, bf16x4 b) { return __builtin_shufflevector(a, b, 0, 1, 2, 3, 4, 5, 6, 7); }
template <class T> DI T lds_ld(u32 a) { return *(LAS T*)(size_t)a; }
template <class T> DI void lds_st(u32 a, T v) { *(LAS T*)(size_t)a = v; }
DI void unpack8(u32x4 w, float* f) {
#pragma unroll
  for (int i = 0; i < 4; ++i) { u32 x = w[i]; f[2 * i] = bflo(x); f[2 * i + 1] = bfhi(x); }
}

__global__ __launch_bounds__(256) void k1_prep(const void* __restrict__ k, const void* __restrict__ v, const void* __restrict__ shk,
                                               const void* __restrict__ c, const void* __restrict__ wmod, char* __restrict__ ws) {
  __shared__ __attribute__((aligned(16))) char smem[64 * 320];
  const u32 sb = (u32)(size_t)(LAS char*)smem;
  const int t = threadIdx.x, b = blockIdx.x;
  STAMP(0, 0);
  if (b < 256) {
    const int dblk = b & 7, sblk = b >> 3;
    const int a = t >> 4, cc = t & 15;
    const int s0 = sblk * 64 + a * 4, ch0 = dblk * 128 + cc * 8;
    rsrc_t rk = mkrsrc(k, S_ * D_ * 2), rv = mkrsrc(v, S_ * D_ * 2), rs = mkrsrc(shk, D_ * 4 * 2);
    u32x4 kk[7], vv[4], sh[4];
#pragma unroll
    for (int j = 0; j < 7; ++j) kk[j] = ldb128(rk, (u32)((s0 - 3 + j) * 2048 + ch0 * 2), AUX_IN);
#pragma unroll
    for (int j = 0; j < 4; ++j) vv[j] = ldb128(rv, (u32)((s0 + j) * 2048 + ch0 * 2), AUX_IN);
#pragma unroll
    for (int j = 0; j < 4; ++j) sh[j] = ldb128(rs, (u32)(ch0 * 8 + j * 16), 0);
    float shf[8][4];
#pragma unroll
    for (int j = 0; j < 4; ++j) { float f[8]; unpack8(sh[j], f);
#pragma unroll
      for (int i = 0; i < 8; ++i) shf[2 * j + (i >> 2)][i & 3] = f[i]; }
    float kf[7][8];
#pragma unroll
    for (int j = 0; j < 7; ++j) unpack8(kk[j], kf[j]);
#pragma unroll
    for (int r = 0; r < 4; ++r) {
      float vf[8], o[8]; unpack8(vv[r], vf);
#pragma unroll
      for (int e = 0; e < 8; ++e) {
        float x = kf[r][e] * shf[e][3];
        x = fmaf(kf[r + 1][e], shf[e][2], x);
        x = fmaf(kf[r + 2][e], shf[e][1], x);
        x = fmaf(kf[r + 3][e], shf[e][0], x);
        o[e] = x * vf[e];
      }
      u32x4 w = {pk2(o[0], o[1]), pk2(o[2], o[3]), pk2(o[4], o[5]), pk2(o[6], o[7])};
      lds_st<u32x4>(sb + (a * 4 + r) * 320 + cc * 16, w);
    }
    STAMP(0, 1);
    __syncthreads();
    const int w = t >> 6, l = t & 63, g = l >> 4, q = (l & 15) >> 2, p = l & 3;
    u32x4* dst = (u32x4*)(ws + WS_KVT);
#pragma unroll
    for (int i = 0; i < 4; ++i) {
      const int u = w * 4 + i, s8 = u >> 1, hf = u & 1;
      const u32 ad = sb + (s8 * 8 + q) * 320 + (hf * 64 + g * 16 + p * 4) * 2;
      bf16x8 r = cat8(tr_read(ad), tr_read(ad + 4 * 320));
      dst[(size_t)(sblk * 8 + s8) * 1024 + dblk * 128 + hf * 64 + l] = __builtin_bit_cast(u32x4, r);
    }
  } else {
    const int bb = b - 256, ksl = bb & 7, nb = bb >> 3;
    const int cc = t & 31, rg = t >> 5;
    const int r0 = ksl * 128 + rg * 16;
    rsrc_t rw = mkrsrc(wmod, 1024 * 6144 * 2), rc = mkrsrc(c, 1024 * 2);
    u32x4 wv[16];
#pragma unroll
    for (int j = 0; j < 16; ++j) wv[j] = ldb128(rw, (u32)((r0 + j) * 12288 + nb * 512 + cc * 16), AUX_IN);
    u32x4 c0 = ldb128(rc, r0 * 2, 0), c1 = ldb128(rc, r0 * 2 + 16, 0);
    float cf[16]; unpack8(c0, cf); unpack8(c1, cf + 8);
    float acc[8] = {0, 0, 0, 0, 0, 0, 0, 0};
#pragma unroll
    for (int j = 0; j < 16; ++j) {
      const float sc = bfr(silu_f(cf[j]));
      float f[8]; unpack8(wv[j], f);
#pragma unroll
      for (int e = 0; e < 8; ++e) acc[e] = fmaf(sc, f[e], acc[e]);
    }
    lds_st<f32x4>(sb + (rg * 256 + cc * 8) * 4, f32x4{acc[0], acc[1], acc[2], acc[3]});
    lds_st<f32x4>(sb + (rg * 256 + cc * 8) * 4 + 16, f32x4{acc[4], acc[5], acc[6], acc[7]});
    __syncthreads();
    float sum = 0.f;
#pragma unroll
    for (int j = 0; j < 8; ++j) sum += lds_ld<float>(sb + (j * 256 + t) * 4);
    ((float*)(ws + WS_MODP))[ksl * 6144 + nb * 256 + t] = sum;
  }
  STAMP(0, 7);
}

constexpr int U_STR = 4576, C_STR = 4160, C_CH = 4 * C_STR, C_BASE = 4 * U_STR;
constexpr int K2_LDS = C_BASE + 4 * C_CH;
__global__ __launch_bounds__(256) void k2_conv(const void* __restrict__ ssmk, const void* __restrict__ ssmd, char* __restrict__ ws) {
  extern __shared__ __attribute__((aligned(16))) char dsm[];
  const u32 sb = (u32)(size_t)(LAS char*)dsm;
  const int t = threadIdx.x, b = blockIdx.x;
  STAMP(1, 0);
  const int cg = (b & 7) * 32 + (b >> 3);
  const int w = __builtin_amdgcn_readfirstlane(t >> 6), l = t & 63;
  const int ch = cg * 4 + w;
  rsrc_t rk = mkrsrc(ssmk, D_ * S_ * 2), ru = mkrsrc(ws + WS_KVT, S_ * D_ * 2);
  u32x4 kw[4], uw[4]; u32x2 kh[4];
#pragma unroll
  for (int q = 0; q < 4; ++q) {
    const u32 o = (u32)(ch * 4096 + (l + 64 * q) * 16);
    kw[q] = ldb128(rk, o, AUX_IN);
    kh[q] = ldb64(rk, o - 8, AUX_IN);
  }
#pragma unroll
  for (int q = 0; q < 4; ++q) uw[q] = ldb128(ru, (u32)((((t >> 2) + 64 * q) * 1024 + cg * 4 + (t & 3)) * 16), 0);
  const float dval = bflo(((const unsigned short*)ssmd)[ch]);
  if (t < 24) {
    const int n = b * 24 + t; float sum = 0.f;
    const float* mp = (const float*)(ws + WS_MODP);
#pragma unroll
    for (int j = 0; j < 8; ++j) sum += mp[j * 6144 + n];
    sum = bfr(sum);
    const int which = n >> 10;
    if (which == 1 || which == 4) sum = bfr(1.f + sum);
    ((float*)(ws + WS_MODF))[n] = sum;
  }
  const u32x4 z4 = {0, 0, 0, 0};
  if (l < 30) lds_st<u32x4>(sb + w * U_STR + l * 16, z4);
  if (l < 8) lds_st<u32x4>(sb + C_BASE + w * C_CH + (l >> 1) * C_STR + 4096 + (l & 1) * 16, z4);
  if (l == 0) kh[0] = u32x2{0, 0};
#pragma unroll
  for (int q = 0; q < 4; ++q) {
    const u32 wm2 = kh[q][0], wm1 = kh[q][1], w0 = kw[q][0], w1 = kw[q][1], w2 = kw[q][2], w3 = kw[q][3];
    const u32 sm1 = __builtin_amdgcn_perm(wm1, wm1, 0x01000302u), s0 = __builtin_amdgcn_perm(w0, w0, 0x01000302u), s1 = __builtin_amdgcn_perm(w1, w1, 0x01000302u),
              s2 = __builtin_amdgcn_perm(w2, w2, 0x01000302u), s3 = __builtin_amdgcn_perm(w3, w3, 0x01000302u);
    const u32 cm1 = __builtin_amdgcn_perm(wm1, wm2, 0x03020504u), c0 = __builtin_amdgcn_perm(w0, wm1, 0x03020504u), c1 = __builtin_amdgcn_perm(w1, w0, 0x03020504u),
              c2 = __builtin_amdgcn_perm(w2, w1, 0x03020504u), c3 = __builtin_amdgcn_perm(w3, w2, 0x03020504u);
    const u32 ad = sb + C_BASE + w * C_CH + (255 - (l + 64 * q)) * 16;
    lds_st<u32x4>(ad + 0 * C_STR, u32x4{c2, c1, c0, cm1});
    lds_st<u32x4>(ad + 1 * C_STR, u32x4{s2, s1, s0, sm1});
    lds_st<u32x4>(ad + 2 * C_STR, u32x4{c3, c2, c1, c0});
    lds_st<u32x4>(ad + 3 * C_STR, u32x4{s3, s2, s1, s0});
  }
#pragma unroll
  for (int q = 0; q < 4; ++q) lds_st<u32x4>(sb + (t & 3) * U_STR + 480 + ((t >> 2) + 64 * q) * 16, uw[q]);
  STAMP(1, 1);
  __syncthreads();
  STAMP(1, 2);

  const int r = l & 15, g = l >> 4;
  const u32 aA = sb + C_BASE + w * C_CH + (r & 3) * C_STR + 24 - 8 * (r >> 2) + 16 * g;
  const u32 aB = sb + w * U_STR + 32 * r + 16 * g;
  f32x4 acc[8];
#pragma unroll
  for (int i = 0; i < 8; ++i) acc[i] = f32x4{0, 0, 0, 0};
  bf16x8 A[2][8], B[2][8];
#define LOADF(X, T0)                                                                                            \
  _Pragma("unroll") for (int i = 0; i < 8; ++i) {                                                               \
    B[X][i] = lds_ld<bf16x8>(aB + 512 * i + 64 * (7 - (T0)));                                                   \
    A[X][i] = cat8(lds_ld<bf16x4>(aA + 64 * (63 - 8 * i - (T0))), lds_ld<bf16x4>(aA + 64 * (63 - 8 * i - (T0)) + 8)); \
  }
#define MMAF(X)                                                                                                 \
  _Pragma("unroll") for (int n = 0; n < 8; ++n)                                                                 \
  _Pragma("unroll") for (int i = 0; i <= n; ++i)                                                                \
    acc[n] = __builtin_amdgcn_mfma_f32_16x16x32_bf16(A[X][i], B[X][n - i], acc[n], 0, 0, 0);
  LOADF(0, 0)
#pragma unroll
  for (int t0 = 0; t0 < 8; t0 += 2) {
    LOADF(1, t0 + 1)
    MMAF(0)
    if (t0 + 2 < 8) { LOADF(0, t0 + 2) }
    MMAF(1)
  }
#undef LOADF
#undef MMAF
  STAMP(1, 3);
#pragma unroll
  for (int n = 0; n < 8; ++n) {
    const u32 ad = sb + w * U_STR + 480 + 512 * n + 32 * r + 8 * g;
    const u32x2 u = lds_ld<u32x2>(ad);
    const u32 u0 = u[0], u1 = u[1];
    const u32 y01 = pk2(acc[n][0], acc[n][1]), y23 = pk2(acc[n][2], acc[n][3]);
    const u32 d01 = pk2(bflo(u0) * dval, bfhi(u0) * dval), d23 = pk2(bflo(u1) * dval, bfhi(u1) * dval);
    lds_st<u32x2>(ad, u32x2{pk2(bflo(y01) + bflo(d01), bfhi(y01) + bfhi(d01)), pk2(bflo(y23) + bflo(d23), bfhi(y23) + bfhi(d23))});
  }
  __syncthreads();
  {
    const int q = (l & 15) >> 2, p = l & 3;
    u32x2* dst = (u32x2*)(ws + WS_YB) + (size_t)cg * 2048;
#pragma unroll
    for (int j = 0; j < 8; ++j) {
      const int s0 = w * 512 + j * 64;
      bf16x4 y = tr_read(sb + q * U_STR + 480 + (s0 + g * 16 + p * 4) * 2);
      dst[s0 + l] = __builtin_bit_cast(u32x2, y);
    }
  }
  STAMP(1, 7);
}

DI float red8(float x) {
  x += __shfl_xor(x, 8); x += __shfl_xor(x, 16); x += __shfl_xor(x, 32); return x;
}
DI void row_stats(const float (&x)[16], u32 slot, int w, int l, float& mean, float& rstd) {
  float s = 0.f;
#pragma unroll
  for (int e = 0; e < 16; ++e) s += x[e];
  const float mw = red8(s) * (1.f / 128.f);
  float qv = 0.f;
#pragma unroll
  for (int e = 0; e < 16; ++e) { const float d = x[e] - mw; qv = fmaf(d, d, qv); }
  qv = red8(qv);
  if (l < 8) lds_st<f32x2>(slot + (l * 8 + w) * 8, f32x2{mw, qv});
  __syncthreads();
  f32x4 p[4];
#pragma unroll
  for (int i = 0; i < 4; ++i) p[i] = lds_ld<f32x4>(slot + (l & 7) * 64 + i * 16);
  float m = 0.f;
#pragma unroll
  for (int i = 0; i < 4; ++i) m += p[i][0] + p[i][2];
  m *= 0.125f;
  float m2 = 0.f, dd = 0.f;
#pragma unroll
  for (int i = 0; i < 4; ++i) { m2 += p[i][1] + p[i][3]; const float d0 = p[i][0] - m, d1 = p[i][2] - m; dd = fmaf(d0, d0, dd); dd = fmaf(d1, d1, dd); }
  mean = m;
  rstd = __builtin_amdgcn_rsqf(fmaf(dd, 128.f, m2) * (1.f / 1024.f) + 1e-5f);
}
__global__ __launch_bounds__(512) void k3_norm(const void* __restrict__ xin, char* __restrict__ ws) {
  __shared__ __attribute__((aligned(16))) char smem[1024];
  const u32 sb = (u32)(size_t)(LAS char*)smem;
  const int t = threadIdx.x, b = blockIdx.x;
  STAMP(2, 0);
  const int rb = (b & 7) * 32 + (b >> 3);
  const int w = t >> 6, l = t & 63, row = l & 7, cc = l >> 3;
  const int s = rb * 8 + row;
  rsrc_t rx = mkrsrc(xin, S_ * D_ * 2), ry = mkrsrc(ws + WS_YB, S_ * D_ * 2), rm = mkrsrc(ws + WS_MODF, 6144 * 4);
  u32x4 xw[2]; u32x2 yw[2][2]; f32x4 md[5][2][2];
#pragma unroll
  for (int j = 0; j < 2; ++j) {
    const int cj = w * 16 + j * 8 + cc;
    xw[j] = ldb128(rx, (u32)(s * 2048 + cj * 16), AUX_IN);
    yw[j][0] = ldb64(ry, (u32)(((2 * cj) * 2048 + s) * 8), 0);
    yw[j][1] = ldb64(ry, (u32)(((2 * cj + 1) * 2048 + s) * 8), 0);
  }
#pragma unroll
  for (int i = 0; i < 5; ++i)
#pragma unroll
    for (int j = 0; j < 2; ++j)
#pragma unroll
      for (int h = 0; h < 2; ++h) md[i][j][h] = __builtin_bit_cast(f32x4, ldb128(rm, (u32)(i * 4096 + (w * 16 + j * 8 + cc) * 32 + h * 16), 0));
  float x[16], y[16];
  unpack8(xw[0], x); unpack8(xw[1], x + 8);
#pragma unroll
  for (int j = 0; j < 2; ++j) unpack8(u32x4{yw[j][0][0], yw[j][0][1], yw[j][1][0], yw[j][1][1]}, y + 8 * j);
  float mean, rstd;
  STAMP(2, 1);
  row_stats(x, sb, w, l, mean, rstd);
  STAMP(2, 2);
  u32 x1p[8];
  {
    const float nm = -mean * rstd; float x1[16];
#pragma unroll
    for (int e = 0; e < 16; ++e) {
      const float sa = md[0][e >> 3][(e >> 2) & 1][e & 3], ca = md[1][e >> 3][(e >> 2) & 1][e & 3], ga = md[2][e >> 3][(e >> 2) & 1][e & 3];
      const float h = fmaf(fmaf(x[e], rstd, nm), ca, sa);
      x1[e] = fmaf(ga, h * y[e], x[e]);
    }
#pragma unroll
    for (int e = 0; e < 8; ++e) { x1p[e] = pk2(x1[2 * e], x1[2 * e + 1]); x[2 * e] = bflo(x1p[e]); x[2 * e + 1] = bfhi(x1p[e]); }
  }
  const size_t fo = (size_t)(s >> 7) * 32 * 8192 + ((s >> 4) & 7) * 1024 + (s & 15) * 16;
#pragma unroll
  for (int j = 0; j < 2; ++j) {
    const int cj = w * 16 + j * 8 + cc;
    *(u32x4*)(ws + WS_X1F + fo + (cj >> 2) * 8192 + (cj & 3) * 256) = u32x4{x1p[4 * j], x1p[4 * j + 1], x1p[4 * j + 2], x1p[4 * j + 3]};
  }
  row_stats(x, sb + 512, w, l, mean, rstd);
  {
    const float nm = -mean * rstd; float h[16];
#pragma unroll
    for (int e = 0; e < 16; ++e) {
      const float sbv = md[3][e >> 3][(e >> 2) & 1][e & 3], cb = md[4][e >> 3][(e >> 2) & 1][e & 3];
      h[e] = fmaf(fmaf(x[e], rstd, nm), cb, sbv);
    }
#pragma unroll
    for (int j = 0; j < 2; ++j) {
      const int cj = w * 16 + j * 8 + cc;
      *(u32x4*)(ws + WS_H2F + fo + (cj >> 2) * 8192 + (cj & 3) * 256) =
          u32x4{pk2(h[8 * j], h[8 * j + 1]), pk2(h[8 * j + 2], h[8 * j + 3]), pk2(h[8 * j + 4], h[8 * j + 5]), pk2(h[8 * j + 6], h[8 * j + 7])};
    }
  }
  STAMP(2, 7);
}

template <int N> DI void wait_vm() { asm volatile("s_waitcnt vmcnt(%0)" ::"n"(N) : "memory"); }
DI void wg_bar() { asm volatile("s_barrier" ::: "memory"); }
DI void dma16(u32 voff, rsrc_t r, u32 soff, u32 ldsaddr, int aux) {
  if (aux == 2) asm volatile("s_mov_b32 m0, %2\n\ts_nop 0\n\tbuffer_load_dwordx4 %0, %1, %3 offen nt lds" ::"v"(voff), "s"(r), "s"(ldsaddr), "s"(soff) : "memory");
  else asm volatile("s_mov_b32 m0, %2\n\ts_nop 0\n\tbuffer_load_dwordx4 %0, %1, %3 offen lds" ::"v"(voff), "s"(r), "s"(ldsaddr), "s"(soff) : "memory");
}

template <int MODE, int NT_, int KS_, int NS_, int SY_, int LDB_>
struct GC {
  static constexpr int NT = NT_, KS = KS_, NS = NS_, SY = SY_, LDB = LDB_;
  static constexpr int BN = 32 * NT, RS = BN * 2, BST = 32 * RS, SB = 8192 + BST, PB = BST / 1024, NB = KS / SY;
  static constexpr int LDS = NS * SB;
  static constexpr int mn(int a, int b) { return a < b ? a : b; }
  static constexpr int need(int j) { return mn(SY * (j + 1), KS - 1); }
  static constexpr int issued(int j) { return j <= 0 ? mn(NS - 1, KS - 1) : mn(SY * (j - 1) + NS, KS - 1); }
  static DI int swz(int row) { return MODE == 1 ? ((row >> 3) & 1) : (RS == 128 ? ((row >> 1) & 3) : (row & 7)); }
};

template <class C, int NPB> struct Loader {
  u32 voffA, voffB[NPB]; rsrc_t rA, rB; u32 soffA, soffB, ldsA, ldsB;
  template <int ST> DI void stage() {
    constexpr int slot = ST % C::NS;
#pragma unroll
    for (int i = 0; i < 2; ++i) dma16(voffA, rA, soffA + ST * 8192 + i * 1024, ldsA + slot * C::SB + i * 1024, 0);
#pragma unroll
    for (int i = 0; i < NPB; ++i) dma16(voffB[i], rB, soffB + ST * 32 * C::LDB, ldsB + slot * C::SB + i * 1024, AUX_W);
  }
  template <int A, int B> DI void stages() { if constexpr (A <= B) { stage<A>(); stages<A + 1, B>(); } }
  template <int J> DI void step() {
    if constexpr (J < C::NB) {
      wait_vm<(C::issued(J) - C::need(J)) * (2 + NPB)>();
      wg_bar();
      if constexpr (J >= 0) stages<C::issued(J) + 1, C::issued(J + 1)>();
      step<J + 1>();
    }
  }
  DI void run(int lw, int pb0, int l, u32 sb, const void* Aptr, u32 Abytes, u32 aoff, const void* Bptr, u32 Bbytes, u32 boff) {
    rA = mkrsrc(Aptr, Abytes); rB = mkrsrc(Bptr, Bbytes);
    voffA = l * 16; soffA = aoff + lw * 2048; ldsA = sb + lw * 2048;
    soffB = boff; ldsB = sb + 8192 + pb0 * 1024;
#pragma unroll
    for (int i = 0; i < NPB; ++i) {
      const int o = (pb0 + i) * 1024 + l * 16, row = o / C::RS, cb = o % C::RS;
      voffB[i] = row * C::LDB + ((((cb >> 5) ^ C::swz(row)) << 5) | (cb & 31));
    }
    stages<0, C::issued(0)>();
    step<-1>();
  }
};

template <class C, int MODE> struct Consumer {
  static constexpr int NBASE = MODE == 1 ? 2 : C::NT;
  f32x4 acc[4][C::NT];
  bf16x8 Af[2][4], Bf[2][C::NT];
  u32 a0, b0[NBASE];
  template <int X> DI void rd(u32 so) {
    const u32 a = a0 + so;
    u32 bb[NBASE];
#pragma unroll
    for (int i = 0; i < NBASE; ++i) bb[i] = b0[i] + so;
    constexpr int TOFF = (MODE == 1 ? 4 : 16) * C::RS;
#pragma unroll
    for (int j = 0; j < C::NT; ++j) {
      const u32 ad = MODE == 1 ? bb[j & 1] + 32 * j : bb[j % NBASE];
      Bf[X][j] = cat8(tr_read(ad), tr_read(ad + TOFF));
      if (j < 4) Af[X][j] = lds_ld<bf16x8>(a + j * 1024);
    }
#pragma unroll
    for (int j = C::NT; j < 4; ++j) Af[X][j] = lds_ld<bf16x8>(a + j * 1024);
  }
  template <int X> DI void mma() {
#pragma unroll
    for (int j = 0; j < C::NT; ++j)
#pragma unroll
      for (int m = 0; m < 4; ++m) acc[m][j] = __builtin_amdgcn_mfma_f32_16x16x32_bf16(Bf[X][j], Af[X][m], acc[m][j], 0, 0, 0);
  }
  DI void sched() {
    constexpr int NR = 4 + 2 * C::NT, NM = 4 * C::NT;
    constexpr int RPM = (NR + NM - 1) / NM;
#pragma unroll
    for (int i = 0; i < NM; ++i) {
      __builtin_amdgcn_sched_group_barrier(0x008, 1, 0);
      __builtin_amdgcn_sched_group_barrier(0x100, RPM, 0);
    }
  }
  DI void sync() {
    asm volatile("s_waitcnt lgkmcnt(0)" ::: "memory");
    __builtin_amdgcn_sched_barrier(0);
    wg_bar();
    __builtin_amdgcn_sched_barrier(0);
  }
  DI void run(int wm, int wc, int l, u32 sb, char* ws, int kid) {
    const int g = l >> 4, q = (l & 15) >> 2, p = l & 3;
    a0 = sb + wm * 4096 + l * 16;
    if constexpr (MODE == 1) {
      const int base = sb + 8192 + (8 * g + q) * C::RS + 8 * p + 32 * C::NT * wc;
      const int dl = (g & 1) * 32;
      b0[0] = base + ((wc & C::NT & 1) ? -dl : dl);
      b0[1] = base + ((wc & C::NT & 1) ? dl : -dl);
    } else {
      const int row = 4 * g + q;
#pragma unroll
      for (int j = 0; j < C::NT; ++j) b0[j] = sb + 8192 + row * C::RS + 8 * p + 32 * ((C::NT * wc + j) ^ C::swz(row));
    }
#pragma unroll
    for (int m = 0; m < 4; ++m)
#pragma unroll
      for (int j = 0; j < C::NT; ++j) acc[m][j] = f32x4{0, 0, 0, 0};
    wg_bar();
    __builtin_amdgcn_sched_barrier(0);
    STAMP(kid, 1);
    rd<0>(0);
    u32 so = C::SB;
    static_assert(C::SY % 2 == 0 && C::KS % C::SY == 0, "steps");
#pragma unroll 1
    for (int j = 0; j < C::NB - 1; ++j) {
      sync();
#pragma unroll
      for (int u = 0; u < C::SY; u += 2) {
        rd<1>(so); mma<0>(); sched();
        so += C::SB; if (so == C::LDS) so = 0;
        __builtin_amdgcn_sched_barrier(0);
        rd<0>(so); mma<1>(); sched();
        so += C::SB; if (so == C::LDS) so = 0;
        __builtin_amdgcn_sched_barrier(0);
      }
    }
    sync();
#pragma unroll
    for (int u = 0; u < C::SY; u += 2) {
      rd<1>(so); mma<0>(); sched();
      so += C::SB; if (so == C::LDS) so = 0;
      __builtin_amdgcn_sched_barrier(0);
      if (u + 2 < C::SY) { rd<0>(so); mma<1>(); sched(); so += C::SB; if (so == C::LDS) so = 0; }
      else mma<1>();
      __builtin_amdgcn_sched_barrier(0);
    }
  }
};

typedef GC<1, 7, 32, 7, 2, F_ * 2> G1;
__global__ __launch_bounds__(512, 2) void k4_up(const void* __restrict__ w1, char* __restrict__ ws) {
  extern __shared__ __attribute__((aligned(16))) char dsm[];
  const u32 sb = (u32)(size_t)(LAS char*)dsm;
  const int t = threadIdx.x, b = blockIdx.x, w = __builtin_amdgcn_readfirstlane(t >> 6), l = t & 63;
  const int x = b & 7, idx = b >> 3;
  const int pm = (x & 1) * 8 + (idx & 7), pn = (x >> 1) * 4 + (idx >> 3);
  STAMP(3, 0);
  if (w >= 4) {
    const int lw = w - 4;
    if (lw < 2) { Loader<G1, 4> L; L.run(lw, lw * 4, l, sb, ws + WS_H2F, S_ * D_ * 2, pm * 32 * 8192, w1, D_ * F_ * 2, pn * 448); }
    else        { Loader<G1, 3> L; L.run(lw, 8 + (lw - 2) * 3, l, sb, ws + WS_H2F, S_ * D_ * 2, pm * 32 * 8192, w1, D_ * F_ * 2, pn * 448); }
    STAMP(3, 7);
    return;
  }
  const int wm = w >> 1, wc = w & 1;
  Consumer<G1, 1> c;
  c.run(wm, wc, l, sb, ws, 3);
  STAMP(3, 2);
  char* hb = ws + WS_HF + ((size_t)(pm * 112 + pn * 7) * 8 + wm * 4) * 1024 + l * 16;
#pragma unroll
  for (int m = 0; m < 4; ++m) {
    u32x2 o[7];
#pragma unroll
    for (int j = 0; j < 7; ++j) { const f32x4 a = c.acc[m][j]; o[j] = u32x2{pk2(silu_f(a[0]), silu_f(a[1])), pk2(silu_f(a[2]), silu_f(a[3]))}; }
    if (wc == 0) {
#pragma unroll
      for (int j = 0; j < 3; ++j) *(u32x4*)(hb + j * 8192 + m * 1024) = u32x4{o[2 * j][0], o[2 * j][1], o[2 * j + 1][0], o[2 * j + 1][1]};
      *(u32x2*)(hb + 3 * 8192 + m * 1024) = o[6];
    } else {
      *(u32x2*)(hb + 3 * 8192 + m * 1024 + 8) = o[0];
#pragma unroll
      for (int j = 0; j < 3; ++j) *(u32x4*)(hb + (4 + j) * 8192 + m * 1024) = u32x4{o[2 * j + 1][0], o[2 * j + 1][1], o[2 * j + 2][0], o[2 * j + 2][1]};
    }
  }
  STAMP(3, 7);
}

typedef GC<2, 2, 112, 12, 4, D_ * 2> G2;
__global__ __launch_bounds__(512, 2) void k5_down(const void* __restrict__ w2, char* __restrict__ ws, unsigned short* __restrict__ out) {
  extern __shared__ __attribute__((aligned(16))) char dsm[];
  const u32 sb = (u32)(size_t)(LAS char*)dsm;
  const int t = threadIdx.x, b = blockIdx.x, w = __builtin_amdgcn_readfirstlane(t >> 6), l = t & 63;
  const int x = b & 7, idx = b >> 3;
  const int pm = (x & 3) * 4 + (idx & 3), pn = (x >> 2) * 8 + (idx >> 2);
  STAMP(4, 0);
  if (w >= 4) {
    const int lw = w - 4;
    Loader<G2, 1> L; L.run(lw, lw, l, sb, ws + WS_HF, S_ * F_ * 2, pm * 112 * 8192, w2, F_ * D_ * 2, pn * 128);
    STAMP(4, 7);
    return;
  }
  const int wm = w >> 1, wc = w & 1, g = l >> 4;
  f32x4 gb[2]; u32x2 xr[4][2];
#pragma unroll
  for (int j = 0; j < 2; ++j) {
    const int jt = pn * 4 + wc * 2 + j;
    gb[j] = *(const f32x4*)(ws + WS_MODF + (5 * 1024 + jt * 16 + 4 * g) * 4);
#pragma unroll
    for (int m = 0; m < 4; ++m)
      xr[m][j] = *(const u32x2*)(ws + WS_X1F + ((size_t)(pm * 32 + (jt >> 1)) * 8 + wm * 4 + m) * 1024 + ((2 * (jt & 1) + (g >> 1)) * 16 + (l & 15)) * 16 + (g & 1) * 8);
  }
  Consumer<G2, 2> c;
  c.run(wm, wc, l, sb, ws, 4);
  STAMP(4, 2);
#pragma unroll
  for (int m = 0; m < 4; ++m)
#pragma unroll
    for (int j = 0; j < 2; ++j) {
      const f32x4 a = c.acc[m][j]; const u32 x0 = xr[m][j][0], x1 = xr[m][j][1];
      const u32 o01 = pk2(a[0], a[1]), o23 = pk2(a[2], a[3]);
      const u32x2 r = {pk2(fmaf(gb[j][0], bflo(o01), bflo(x0)), fmaf(gb[j][1], bfhi(o01), bfhi(x0))),
                       pk2(fmaf(gb[j][2], bflo(o23), bflo(x1)), fmaf(gb[j][3], bfhi(o23), bfhi(x1)))};
      *(u32x2*)(out + (size_t)(pm * 128 + wm * 64 + m * 16 + (l & 15)) * 1024 + (pn * 4 + wc * 2 + j) * 16 + 4 * g) = r;
    }
  STAMP(4, 7);
}

__global__ __launch_bounds__(512) void k0_pre(const void* a0,const void* a1,const void* a2,const void* a4,const void* a7,const void* a8,const void* a10, char* ws){
  const int t = blockIdx.x*512+threadIdx.x;
  u32x4 acc={0,0,0,0};
  const void* ps[7]={a0,a1,a2,a4,a7,a8,a10};
  const u32 by[7]={4u<<20,4u<<20,4u<<20,4u<<20,7340032u,7340032u,12582912u};
#pragma unroll
  for(int i=0;i<7;++i){ rsrc_t r=mkrsrc(ps[i],by[i]);
#pragma unroll
    for(u32 o=0;o<by[i];o+=(2u<<20)) acc^=ldb128(r,o+t*16,PREAUX); }
  if(acc[0]==0x12345678u&&acc[1]==0x9abcdef0u&&acc[2]==0x13572468u) *(u32*)(ws+(100u<<20))=acc[3];
}
extern "C" void kernel_launch(void* const* d_in, const int* in_sizes, int n_in, void* d_out, int out_size, void* d_ws, size_t ws_size,
                              hipStream_t stream) {
  char* ws = (char*)d_ws;
  (void)hipFuncSetAttribute((const void*)k2_conv, hipFuncAttributeMaxDynamicSharedMemorySize, K2_LDS);
  (void)hipFuncSetAttribute((const void*)k4_up, hipFuncAttributeMaxDynamicSharedMemorySize, G1::LDS);
  (void)hipFuncSetAttribute((const void*)k5_down, hipFuncAttributeMaxDynamicSharedMemorySize, G2::LDS);
  k0_pre<<<256,512,0,stream>>>(d_in[0],d_in[1],d_in[2],d_in[4],d_in[7],d_in[8],d_in[10],ws);
  k1_prep<<<448, 256, 0, stream>>>(d_in[1], d_in[2], d_in[3], d_in[9], d_in[10], ws);
  k2_conv<<<256, 256, K2_LDS, stream>>>(d_in[4], d_in[5], ws);
  k3_norm<<<256, 512, 0, stream>>>(d_in[0], ws);
  k4_up<<<256, 512, G1::LDS, stream>>>(d_in[7], ws);
  k5_down<<<256, 512, G2::LDS, stream>>>(d_in[8], ws, (unsigned short*)d_out);
}
```

```cpp
#include <hip/hip_runtime.h>
#include <hip/hip_bf16.h>

typedef unsigned u32;
typedef unsigned u32x2 __attribute__((ext_vector_type(2)));
typedef unsigned u32x3 __attribute__((ext_vector_type(3)));
typedef unsigned u32x4 __attribute__((ext_vector_type(4)));
typedef float f32x2 __attribute__((ext_vector_type(2)));
typedef float f32x4 __attribute__((ext_vector_type(4)));
typedef __bf16 bf16x2 __attribute__((ext_vector_type(2)));
typedef __bf16 bf16x4 __attribute__((ext_vector_type(4)));
typedef __bf16 bf16x8 __attribute__((ext_vector_type(8)));
#define LAS __attribute__((address_space(3)))
#define GAS __attribute__((address_space(1)))
#define DI __device__ __forceinline__
typedef __amdgpu_buffer_rsrc_t rsrc_t;

constexpr int S_ = 2048, D_ = 1024, F_ = 3584;
#define AUX_IN 0
constexpr int SC1 = 16;

constexpr size_t WS_KVT = 0;
constexpr size_t WS_YB = 4u << 20;
constexpr size_t WS_X1F = 8u << 20;
constexpr size_t WS_H2F = 12u << 20;
constexpr size_t WS_HF = 16u << 20;
constexpr size_t WS_XCH = 32u << 20;
constexpr size_t WS_MOD = 40u << 20;
constexpr size_t WS_FLG = 41u << 20;
constexpr int FL_A2A = 0, FL_LN = 64, FL_G1 = 128, FL_XC = 192, FL_LINES = 192 + 256;

#define STAMP(kid, slot)

DI rsrc_t mkrsrc(const void* p, u32 bytes) { return __builtin_amdgcn_make_buffer_rsrc((void*)p, 0, bytes, 0x00020000); }
DI float bflo(u32 w) { return __uint_as_float(w << 16); }
DI float bfhi(u32 w) { return __uint_as_float(w & 0xffff0000u); }
DI u32 pk2(float a, float b) { f32x2 v = {a, b}; bf16x2 r = __builtin_convertvector(v, bf16x2); return __builtin_bit_cast(u32, r); }
DI float bfr(float a) { return bflo(pk2(a, 0.f)); }
DI float silu_f(float x) { return x * __builtin_amdgcn_rcpf(1.f + __builtin_amdgcn_exp2f(x * -1.44269504089f)); }
DI u32 silu_pk(float a, float b) {
  const f32x2 v = {a, b}, t = v * -1.44269504089f;
  f32x2 e = {__builtin_amdgcn_exp2f(t[0]), __builtin_amdgcn_exp2f(t[1])};
  e = e + 1.f;
  const f32x2 r = {__builtin_amdgcn_rcpf(e[0]), __builtin_amdgcn_rcpf(e[1])}, o = v * r;
  return pk2(o[0], o[1]);
}
template <int AUX> DI u32x4 ld128(rsrc_t r, u32 off) { return __builtin_amdgcn_raw_buffer_load_b128(r, off, 0, AUX); }
template <int AUX> DI u32x3 ld96(rsrc_t r, u32 off) { return __builtin_amdgcn_raw_buffer_load_b96(r, off, 0, AUX); }
template <int AUX> DI u32x2 ld64(rsrc_t r, u32 off) { return __builtin_amdgcn_raw_buffer_load_b64(r, off, 0, AUX); }
template <int AUX> DI void st128(rsrc_t r, u32 off, u32x4 v) { __builtin_amdgcn_raw_buffer_store_b128(v, r, off, 0, AUX); }
template <int AUX> DI void st64(rsrc_t r, u32 off, u32x2 v) { __builtin_amdgcn_raw_buffer_store_b64(v, r, off, 0, AUX); }
template <int AUX> DI void st32(rsrc_t r, u32 off, u32 v) { __builtin_amdgcn_raw_buffer_store_b32(v, r, off, 0, AUX); }
DI bf16x4 tr_read(u32 a) { return __builtin_amdgcn_ds_read_tr16_b64_v4bf16((LAS bf16x4*)(size_t)a); }
DI bf16x8 cat8(bf16x4 a, bf16x4 b) { return __builtin_shufflevector(a, b, 0, 1, 2, 3, 4, 5, 6, 7); }
template <class T> DI T lds_ld(u32 a) { return *(LAS T*)(size_t)a; }
template <class T> DI void lds_st(u32 a, T v) { *(LAS T*)(size_t)a = v; }
DI void unpack8(u32x4 w, float* f) {
#pragma unroll
  for (int i = 0; i < 4; ++i) { u32 x = w[i]; f[2 * i] = bflo(x); f[2 * i + 1] = bfhi(x); }
}
template <int N> DI void wait_vm() { asm volatile("s_waitcnt vmcnt(%0)" ::"n"(N) : "memory"); }
DI void wg_bar() { asm volatile("s_barrier" ::: "memory"); }
DI void lds_bar() { asm volatile("s_waitcnt lgkmcnt(0)\n\ts_barrier" ::: "memory"); }
template <int CTRL> DI float dpp_add(float x) {
  return x + __int_as_float(__builtin_amdgcn_update_dpp(0, __float_as_int(x), CTRL, 0xf, 0xf, true));
}
DI float add_x16(float x) { const u32 u = __float_as_uint(x); auto r = __builtin_amdgcn_permlane16_swap(u, u, false, false); const u32 a = r[0], b = r[1]; return __uint_as_float(a) + __uint_as_float(b); }
DI float add_x32(float x) { const u32 u = __float_as_uint(x); auto r = __builtin_amdgcn_permlane32_swap(u, u, false, false); const u32 a = r[0], b = r[1]; return __uint_as_float(a) + __uint_as_float(b); }

DI void flag_add(char* ws, int line) { __hip_atomic_fetch_add((GAS u32*)(ws + WS_FLG + (size_t)line * 128), 1u, __ATOMIC_RELAXED, __HIP_MEMORY_SCOPE_AGENT); }
DI void flag_wait(char* ws, u32 byteoff, u32 target) {
  const GAS u32* p = (const GAS u32*)(ws + WS_FLG + byteoff);
  for (u32 spins = 0; spins < (1u << 18); ++spins) {
    const u32 v = __hip_atomic_load(p, __ATOMIC_RELAXED, __HIP_MEMORY_SCOPE_AGENT);
    if (__all(v >= target)) break;
    __builtin_amdgcn_s_sleep(1);
  }
  __builtin_amdgcn_fence(__ATOMIC_ACQUIRE, "wavefront");
  asm volatile("" ::: "memory");
}
DI u32 touch(rsrc_t r, u32 voff) { return __builtin_amdgcn_raw_buffer_load_b32(r, voff, 0, 0); }

__global__ __launch_bounds__(256) void k1_prep(const void* __restrict__ k, const void* __restrict__ v, const void* __restrict__ shk,
                                               const void* __restrict__ ssmk, char* __restrict__ ws) {
  __shared__ __attribute__((aligned(16))) char smem[64 * 320];
  const u32 sb = (u32)(size_t)(LAS char*)smem;
  const int t = threadIdx.x, b = blockIdx.x;
  STAMP(0, 0);
  const int dblk = b & 7, sblk = b >> 3;
  const int a = t >> 4, cc = t & 15;
  const int s0 = sblk * 64 + a * 4, ch0 = dblk * 128 + cc * 8;
  rsrc_t rk = mkrsrc(k, S_ * D_ * 2), rv = mkrsrc(v, S_ * D_ * 2), rs = mkrsrc(shk, D_ * 4 * 2);
  u32x4 kk[7], vv[4], sh[4];
#pragma unroll
  for (int j = 0; j < 7; ++j) kk[j] = ld128<AUX_IN>(rk, (u32)((s0 - 3 + j) * 2048 + ch0 * 2));
#pragma unroll
  for (int j = 0; j < 4; ++j) vv[j] = ld128<AUX_IN>(rv, (u32)((s0 + j) * 2048 + ch0 * 2));
#pragma unroll
  for (int j = 0; j < 4; ++j) sh[j] = ld128<0>(rs, (u32)(ch0 * 8 + j * 16));
  const u32 pf = touch(mkrsrc(ssmk, D_ * S_ * 2), (u32)((dblk * 32 + sblk) * 16384 + (t & 127) * 128));
  __builtin_amdgcn_sched_barrier(0);
  float shf[8][4];
#pragma unroll
  for (int j = 0; j < 4; ++j) { float f[8]; unpack8(sh[j], f);
#pragma unroll
    for (int i = 0; i < 8; ++i) shf[2 * j + (i >> 2)][i & 3] = f[i]; }
  float kf[7][8];
#pragma unroll
  for (int j = 0; j < 7; ++j) unpack8(kk[j], kf[j]);
#pragma unroll
  for (int r = 0; r < 4; ++r) {
    float vf[8], o[8]; unpack8(vv[r], vf);
#pragma unroll
    for (int e = 0; e < 8; ++e) {
      float x = kf[r][e] * shf[e][3];
      x = fmaf(kf[r + 1][e], shf[e][2], x);
      x = fmaf(kf[r + 2][e], shf[e][1], x);
      x = fmaf(kf[r + 3][e], shf[e][0], x);
      o[e] = x * vf[e];
    }
    u32x4 w = {pk2(o[0], o[1]), pk2(o[2], o[3]), pk2(o[4], o[5]), pk2(o[6], o[7])};
    lds_st<u32x4>(sb + (a * 4 + r) * 320 + cc * 16, w);
  }
  STAMP(0, 1);
  __syncthreads();
  const int w = t >> 6, l = t & 63, g = l >> 4, q = (l & 15) >> 2, p = l & 3;
  u32x4* dst = (u32x4*)(ws + WS_KVT);
#pragma unroll
  for (int i = 0; i < 4; ++i) {
    const int u = w * 4 + i, s8 = u >> 1, hf = u & 1;
    const u32 ad = sb + (s8 * 8 + q) * 320 + (hf * 64 + g * 16 + p * 4) * 2;
    bf16x8 r = cat8(tr_read(ad), tr_read(ad + 4 * 320));
    dst[(size_t)(sblk * 8 + s8) * 1024 + dblk * 128 + hf * 64 + l] = __builtin_bit_cast(u32x4, r);
  }
  if (t < 2 && b * 2 + t < FL_LINES) *(u32x4*)(ws + WS_FLG + (size_t)(b * 2 + t) * 128) = u32x4{0, 0, 0, 0};
  asm volatile("" ::"v"(pf));
  STAMP(0, 7);
}

template <int AUX> DI void dma16(u32 voff, rsrc_t r, u32 soff, u32 ldsaddr) {
  if constexpr (AUX == SC1) asm volatile("s_mov_b32 m0, %2\n\ts_nop 0\n\tbuffer_load_dwordx4 %0, %1, %3 offen sc1 lds" ::"v"(voff), "s"(r), "s"(ldsaddr), "s"(soff) : "memory");
  else asm volatile("s_mov_b32 m0, %2\n\ts_nop 0\n\tbuffer_load_dwordx4 %0, %1, %3 offen lds" ::"v"(voff), "s"(r), "s"(ldsaddr), "s"(soff) : "memory");
}

template <int MODE_, int NT_, int KS_, int NS_, int SY_, int LDB_>
struct GC {
  static constexpr int MODE = MODE_, NT = NT_, KS = KS_, NS = NS_, SY = SY_, LDB = LDB_;
  static constexpr int NPB = MODE == 1 ? 4 : 2;
  static constexpr int PSTR = MODE == 1 ? 1088 : 1024, SB = 8192 + 4 * NPB * PSTR, NB = KS / SY, LDS = NS * SB;
  static constexpr int mn(int a, int b) { return a < b ? a : b; }
  static constexpr int need(int j) { return mn(SY * (j + 1), KS - 1); }
  static constexpr int issued(int j) { return j <= 0 ? NS - 1 : mn(SY * (j - 1) + NS, KS - 1); }
  static constexpr int cnt(int a, int b) { int n = 0; for (int s = a; s <= b; ++s) n += s < NS ? 2 : 2 + NPB; return n; }
};

template <class C> struct Loader {
  u32 voffA, voffB; rsrc_t rA, rB; u32 soffA, soffB, ldsA, ldsB;
  template <int ST> DI void stA() {
#pragma unroll
    for (int i = 0; i < 2; ++i) dma16<SC1>(voffA, rA, soffA + ST * 8192 + i * 1024, ldsA + (ST % C::NS) * C::SB + i * 1024);
  }
  template <int ST> DI void stB() {
#pragma unroll
    for (int i = 0; i < C::NPB; ++i) {
      if constexpr (C::MODE == 1) dma16<0>(voffB, rB, soffB + (ST * 32 + 2 * i) * C::LDB, ldsB + (ST % C::NS) * C::SB + ((i & 1) + 4 * (i >> 1)) * C::PSTR);
      else dma16<0>(voffB ^ (i * 128), rB, soffB + (ST * 32 + 4 * i) * C::LDB, ldsB + (ST % C::NS) * C::SB + i * 1024);
    }
  }
  template <int A, int B, int WHAT> DI void stages() { if constexpr (A <= B) { if constexpr (WHAT & 1) stA<A>(); if constexpr (WHAT & 2) stB<A>(); stages<A + 1, B, WHAT>(); } }
  template <int J> DI void step() {
    if constexpr (J < C::NB) {
      wait_vm<C::cnt(C::need(J) + 1, C::issued(J))>();
      wg_bar();
      if constexpr (J >= 0) stages<C::issued(J) + 1, C::issued(J + 1), 3>();
      step<J + 1>();
    }
  }
  DI void init(int lw, int l, u32 sb, const void* Aptr, u32 Abytes, u32 aoff, const void* Bptr, u32 Bbytes, u32 boff) {
    rA = mkrsrc(Aptr, Abytes); rB = mkrsrc(Bptr, Bbytes);
    voffA = l * 16; soffA = aoff + lw * 2048; ldsA = sb + lw * 2048;
    soffB = boff + lw * 8 * C::LDB;
    if constexpr (C::MODE == 1) {
      const int h = l >> 5;
      voffB = h * C::LDB + (((l & 31) * 16) ^ (h * 32));
      ldsB = sb + 8192 + (2 * (lw & 1) + 8 * (lw >> 1)) * C::PSTR;
    } else {
      const int h = l >> 4;
      voffB = h * C::LDB + (((l & 15) * 16) ^ (h * 32));
      ldsB = sb + 8192 + lw * 2048;
    }
  }
  DI void prefetchB() { stages<0, C::NS - 1, 2>(); }
  DI void run() { stages<0, C::NS - 1, 1>(); step<-1>(); }
};

template <class C> struct Consumer {
  static constexpr int NBASE = C::MODE == 1 ? 2 : C::NT;
  f32x4 acc[4][C::NT];
  bf16x8 Af[2][4], Bf[2][C::NT];
  u32 a0, b0[NBASE];
  template <int X> DI void rd(u32 so) {
    const u32 a = a0 + so;
    u32 bb[NBASE];
#pragma unroll
    for (int i = 0; i < NBASE; ++i) bb[i] = b0[i] + so;
    constexpr int TOFF = C::MODE == 1 ? 4 * C::PSTR : 4096;
#pragma unroll
    for (int j = 0; j < C::NT; ++j) {
      const u32 ad = C::MODE == 1 ? bb[j & 1] + 32 * j : bb[j % NBASE];
      Bf[X][j] = cat8(tr_read(ad), tr_read(ad + TOFF));
      if (j == 0) {
#pragma unroll
        for (int m = 0; m < 4; ++m) Af[X][m] = lds_ld<bf16x8>(a + m * 1024);
      }
    }
  }
  template <int X> DI void mma() {
#pragma unroll
    for (int j = 0; j < C::NT; ++j)
#pragma unroll
      for (int m = 0; m < 4; ++m) acc[m][j] = __builtin_amdgcn_mfma_f32_16x16x32_bf16(Bf[X][j], Af[X][m], acc[m][j], 0, 0, 0);
  }
  DI void sched() {
    constexpr int NM = 4 * C::NT, RPM = C::MODE == 1 ? 1 : 2;
#pragma unroll
    for (int i = 0; i < NM; ++i) {
      __builtin_amdgcn_sched_group_barrier(0x008, 1, 0);
      __builtin_amdgcn_sched_group_barrier(0x100, RPM, 0);
    }
  }
  DI void sync() {
    asm volatile("s_waitcnt lgkmcnt(0)" ::: "memory");
    __builtin_amdgcn_sched_barrier(0);
    wg_bar();
    __builtin_amdgcn_sched_barrier(0);
  }
  DI void run(int wm, int wc, int l, u32 sb, int coff, char* ws, int kid) {
    const int g = l >> 4, q = (l & 15) >> 2, p = l & 3;
    a0 = sb + wm * 4096 + l * 16;
    if constexpr (C::MODE == 1) {
      const int base = sb + 8192 + ((q >> 1) + 2 * (g & 1) + 8 * (g >> 1)) * C::PSTR + (q & 1) * 512 + 8 * p + 32 * (C::NT * wc + coff);
      const int dl = (q & 1) * 32;
      b0[0] = base + (wc ? -dl : dl);
      b0[1] = base + (wc ? dl : -dl);
    } else {
      const int row = 4 * g + q;
#pragma unroll
      for (int j = 0; j < C::NT; ++j) b0[j] = sb + 8192 + row * 256 + 8 * p + 32 * ((C::NT * wc + j) ^ (row & 7));
    }
#pragma unroll
    for (int m = 0; m < 4; ++m)
#pragma unroll
      for (int j = 0; j < C::NT; ++j) acc[m][j] = f32x4{0, 0, 0, 0};
    __builtin_amdgcn_sched_barrier(0);
    wg_bar();
    __builtin_amdgcn_sched_barrier(0);
    STAMP(kid, 1);
    rd<0>(0);
    u32 so = C::SB;
    static_assert(C::SY % 2 == 0 && C::KS % C::SY == 0, "steps");
#pragma unroll 1
    for (int j = 0; j < C::NB - 1; ++j) {
      sync();
#pragma unroll
      for (int u = 0; u < C::SY; u += 2) {
        rd<1>(so); mma<0>(); sched();
        so += C::SB; if (so == C::LDS) so = 0;
        __builtin_amdgcn_sched_barrier(0);
        rd<0>(so); mma<1>(); sched();
        so += C::SB; if (so == C::LDS) so = 0;
        __builtin_amdgcn_sched_barrier(0);
      }
    }
    sync();
#pragma unroll
    for (int u = 0; u < C::SY; u += 2) {
      rd<1>(so); mma<0>(); sched();
      so += C::SB; if (so == C::LDS) so = 0;
      __builtin_amdgcn_sched_barrier(0);
      if (u + 2 < C::SY) { rd<0>(so); mma<1>(); sched(); so += C::SB; if (so == C::LDS) so = 0; }
      else { sync(); mma<1>(); }
      __builtin_amdgcn_sched_barrier(0);
    }
  }
};
#define G1_NS 6
#define G1_SY 2
#define G2_NS 9
#define G2_SY 2
typedef GC<1, 7, 32, G1_NS, G1_SY, F_ * 2> G1;
typedef GC<2, 4, 56, G2_NS, G2_SY, D_ * 2> G2;

constexpr int U_STR = 4576, C_STR = 4160, C_CH = 4 * C_STR, C_BASE = 4 * U_STR, GP_BASE = C_BASE + 4 * C_CH;
constexpr int LN_MOD4 = G1::SB, LN_ST = G1::SB + 2048;
constexpr int KP_LDS = G1::LDS > G2::LDS ? G1::LDS : G2::LDS;

DI float red8(float x) { return add_x32(add_x16(dpp_add<0x128>(x))); }
DI void row_stats(const float (&x)[16], u32 slot, int w, int l, float& mean, float& rstd) {
  float s = 0.f;
#pragma unroll
  for (int e = 0; e < 16; ++e) s += x[e];
  const float mw = red8(s) * (1.f / 128.f);
  float qv = 0.f;
#pragma unroll
  for (int e = 0; e < 16; ++e) { const float d = x[e] - mw; qv = fmaf(d, d, qv); }
  qv = red8(qv);
  if (l < 8) lds_st<f32x2>(slot + (l * 8 + w) * 8, f32x2{mw, qv});
  lds_bar();
  f32x4 p[4];
#pragma unroll
  for (int i = 0; i < 4; ++i) p[i] = lds_ld<f32x4>(slot + (l & 7) * 64 + i * 16);
  float m = 0.f;
#pragma unroll
  for (int i = 0; i < 4; ++i) m += p[i][0] + p[i][2];
  m *= 0.125f;
  float m2 = 0.f, dd = 0.f;
#pragma unroll
  for (int i = 0; i < 4; ++i) { m2 += p[i][1] + p[i][3]; const float d0 = p[i][0] - m, d1 = p[i][2] - m; dd = fmaf(d0, d0, dd); dd = fmaf(d1, d1, dd); }
  mean = m;
  rstd = __builtin_amdgcn_rsqf(fmaf(dd, 128.f, m2) * (1.f / 1024.f) + 1e-5f);
}

template <int LO, int HI>
__global__ __launch_bounds__(512, 2) void kp(const void* __restrict__ xin, const void* __restrict__ ssmk, const void* __restrict__ ssmd,
                                             const void* __restrict__ cin, const void* __restrict__ wmod, const void* __restrict__ w1,
                                             const void* __restrict__ w2, char* __restrict__ ws, unsigned short* __restrict__ out) {
  extern __shared__ __attribute__((aligned(16))) char dsm[];
  const u32 sb = (u32)(size_t)(LAS char*)dsm;
  const int t = threadIdx.x, b = blockIdx.x, w = __builtin_amdgcn_readfirstlane(t >> 6), l = t & 63;
  const int x = b & 7, idx = b >> 3, cg = x * 32 + idx;
  const int pm = (x & 1) * 8 + (idx & 7), pn = (x >> 1) * 4 + (idx >> 3), kh = x >> 2;
  const int srow = pm * 128 + pn * 8 + (l & 7), cj0 = w * 16 + (l >> 3);
  u32x4 xw[2];
  Loader<G1> L1; Loader<G2> L2;

  if constexpr (LO <= 0) {
    STAMP(1, 0);
    u32 pf[4] = {0, 0, 0, 0};
    if (w < 4) {
      const int ch = cg * 4 + w;
      rsrc_t rk = mkrsrc(ssmk, D_ * S_ * 2);
      u32x4 kw[4]; u32x2 khl[4];
#pragma unroll
      for (int q = 0; q < 4; ++q) {
        const u32 o = (u32)(ch * 4096 + (l + 64 * q) * 16);
        kw[q] = ld128<0>(rk, o);
        khl[q] = ld64<0>(rk, o - 8);
      }
      u32x4 uw[4];
#pragma unroll
      for (int q = 0; q < 4; ++q) uw[q] = ld128<0>(mkrsrc(ws + WS_KVT, S_ * D_ * 2), (u32)((((t >> 2) + 64 * q) * 1024 + cg * 4 + (t & 3)) * 16));
      const float dval = bflo(((const unsigned short*)ssmd)[ch]);
      if (t == 0) lds_st<u32>(sb + GP_BASE + 1536, 0u);
      __builtin_amdgcn_sched_barrier(0);
      lds_bar();
      __builtin_amdgcn_sched_barrier(0);
      if constexpr (HI > 1) {
#pragma unroll
        for (int j = 0; j < 2; ++j) xw[j] = ld128<AUX_IN>(mkrsrc(xin, S_ * D_ * 2), (u32)(srow * 2048 + (cj0 + j * 8) * 16));
      }
      const u32x4 z4 = {0, 0, 0, 0};
      if (l < 30) lds_st<u32x4>(sb + w * U_STR + l * 16, z4);
      if (l < 8) lds_st<u32x4>(sb + C_BASE + w * C_CH + (l >> 1) * C_STR + (l & 1) * 16, z4);
      if (l == 0) khl[0] = u32x2{0, 0};
#pragma unroll
      for (int q = 0; q < 4; ++q) {
        const u32 wm2 = khl[q][0], wm1 = khl[q][1], w0 = kw[q][0], w1_ = kw[q][1], w2_ = kw[q][2], w3 = kw[q][3];
        const u32 am = __builtin_amdgcn_alignbit(wm1, wm2, 16), a0 = __builtin_amdgcn_alignbit(w0, wm1, 16), a1 = __builtin_amdgcn_alignbit(w1_, w0, 16),
                  a2 = __builtin_amdgcn_alignbit(w2_, w1_, 16), a3 = __builtin_amdgcn_alignbit(w3, w2_, 16);
        const u32 ad = sb + C_BASE + w * C_CH + 32 + (l + 64 * q) * 16;
        lds_st<u32x4>(ad + 0 * C_STR, kw[q]);
        lds_st<u32x4>(ad + 1 * C_STR, u32x4{a0, a1, a2, a3});
        lds_st<u32x4>(ad + 2 * C_STR, u32x4{wm1, w0, w1_, w2_});
        lds_st<u32x4>(ad + 3 * C_STR, u32x4{am, a0, a1, a2});
      }
#pragma unroll
      for (int q = 0; q < 4; ++q) lds_st<u32x4>(sb + (t & 3) * U_STR + 480 + ((t >> 2) + 64 * q) * 16, uw[q]);
      STAMP(1, 1);
      {
        asm volatile("s_waitcnt lgkmcnt(0)" ::: "memory");
        LAS u32* cnt = (LAS u32*)(size_t)(sb + GP_BASE + 1536);
        if (l == 0) __hip_atomic_fetch_add(cnt, 1u, __ATOMIC_RELAXED, __HIP_MEMORY_SCOPE_WORKGROUP);
        while (__builtin_amdgcn_readfirstlane(__hip_atomic_load(cnt, __ATOMIC_RELAXED, __HIP_MEMORY_SCOPE_WORKGROUP)) < 4u) {}
        asm volatile("" ::: "memory");
      }
      STAMP(1, 2);
      const int r = l & 15, g = l >> 4;
      const u32 aA = sb + C_BASE + w * C_CH + (r >> 2) * C_STR + 56 + 8 * (l & 3) - 16 * g;
      const u32 aB = sb + w * U_STR + 32 * r + 16 * g;
      f32x4 acc[8];
#pragma unroll
      for (int n = 0; n < 8; ++n) {
        const u32x2 u = lds_ld<u32x2>(sb + w * U_STR + 480 + 512 * n + 32 * r + 8 * g);
        const u32 u0 = u[0], u1 = u[1];
        acc[n] = f32x4{bflo(u0), bfhi(u0), bflo(u1), bfhi(u1)} * dval;
      }
      bf16x8 A[2][8], B[2][8];
#define LOADF(X, T0)                                                                                        \
  _Pragma("unroll") for (int i = 0; i < 8; ++i) {                                                           \
    B[X][i] = lds_ld<bf16x8>(aB + 512 * i + 64 * (7 - (T0)));                                               \
    A[X][i] = cat8(tr_read(aA + 64 * (8 * i + (T0)) + 8), tr_read(aA + 64 * (8 * i + (T0))));               \
  }
#define MMAF(X)                                                                                             \
  _Pragma("unroll") for (int n = 0; n < 8; ++n)                                                             \
  _Pragma("unroll") for (int i = 0; i <= n; ++i)                                                            \
    acc[n] = __builtin_amdgcn_mfma_f32_16x16x32_bf16(A[X][i], B[X][n - i], acc[n], 0, 0, 0);
#define SCHEDF                                                                                              \
  _Pragma("unroll") for (int i = 0; i < 36; ++i) {                                                          \
    __builtin_amdgcn_sched_group_barrier(0x008, 1, 0);                                                      \
    __builtin_amdgcn_sched_group_barrier(0x100, 1, 0);                                                      \
  }                                                                                                         \
  __builtin_amdgcn_sched_barrier(0);
      LOADF(0, 0)
      __builtin_amdgcn_sched_barrier(0);
#pragma unroll
      for (int t0 = 0; t0 < 8; t0 += 2) {
        LOADF(1, t0 + 1)
        MMAF(0)
        SCHEDF
        if (t0 + 2 < 8) { LOADF(0, t0 + 2) }
        MMAF(1)
        SCHEDF
      }
#undef LOADF
#undef MMAF
#undef SCHEDF
      STAMP(1, 3);
      const u32 ay = sb + C_BASE + w * (C_CH + 64) + 16 * r + 4 * g;
#pragma unroll
      for (int n = 0; n < 8; ++n) {
        lds_st<u32>(ay + 256 * n, pk2(acc[n][0], acc[n][2]));
        lds_st<u32>(ay + 256 * n + 2048, pk2(acc[n][1], acc[n][3]));
      }
    } else {
      const int ta = t - 256, aw = w - 4;
      rsrc_t rw = mkrsrc(wmod, 1024 * 6144 * 2), rc = mkrsrc(cin, 1024 * 2);
      wg_bar();
      __builtin_amdgcn_sched_barrier(0);
      const int r0 = aw * 256 + (l >> 2) * 16;
      const u32x4 c0 = ld128<0>(rc, r0 * 2), c1 = ld128<0>(rc, r0 * 2 + 16);
      u32x3 wv[16];
#pragma unroll
      for (int j = 0; j < 16; ++j) wv[j] = ld96<AUX_IN>(rw, (u32)((r0 + j) * 12288 + cg * 48 + (l & 3) * 12));
      if constexpr (HI > 1) {
#pragma unroll
        for (int j = 0; j < 2; ++j) xw[j] = ld128<AUX_IN>(mkrsrc(xin, S_ * D_ * 2), (u32)(srow * 2048 + (cj0 + j * 8) * 16));
      }
      STAMP(1, 1);
      {
        rsrc_t r1 = mkrsrc(w1, D_ * F_ * 2), r2 = mkrsrc(w2, D_ * F_ * 2);
#pragma unroll
        for (int j = 0; j < 2; ++j) {
          const int u = ta + (aw < 3 ? 256 * j : 0);
          const u32 o1 = (u32)((idx * 32 + u / 14) * (F_ * 2) + (x >> 1) * 1792 + (u % 14) * 128);
          const u32 o2 = (u32)((kh * 1792 + idx * 56 + (u >> 3)) * 2048 + ((x >> 1) & 1) * 1024 + (u & 7) * 128);
          pf[2 * j] = touch(r1, o1); pf[2 * j + 1] = touch(r2, o2);
        }
        __builtin_amdgcn_sched_barrier(0);
      }
      float cf[16]; unpack8(c0, cf); unpack8(c1, cf + 8);
      float acc[6] = {0, 0, 0, 0, 0, 0};
#pragma unroll
      for (int j = 0; j < 16; ++j) {
        const float sc = bfr(silu_f(cf[j]));
#pragma unroll
        for (int e = 0; e < 3; ++e) { const u32 ww = wv[j][e]; acc[2 * e] = fmaf(sc, bflo(ww), acc[2 * e]); acc[2 * e + 1] = fmaf(sc, bfhi(ww), acc[2 * e + 1]); }
      }
#pragma unroll
      for (int e = 0; e < 6; ++e) acc[e] = dpp_add<0x128>(dpp_add<0x124>(acc[e]));
      if ((l & 12) == 0) {
#pragma unroll
        for (int e = 0; e < 3; ++e) lds_st<f32x2>(sb + GP_BASE + ((aw * 4 + (l >> 4)) * 24 + (l & 3) * 6 + 2 * e) * 4, f32x2{acc[2 * e], acc[2 * e + 1]});
      }
      STAMP(1, 3);
    }
    lds_bar();
    STAMP(1, 4);
    {
      const int g = l >> 4, q = (l & 15) >> 2, p = l & 3;
      rsrc_t ry = mkrsrc(ws + WS_YB, S_ * D_ * 2);
#pragma unroll
      for (int i = 0; i < 2; ++i) {
        const int u = w * 2 + i;
        const u32 ad = sb + C_BASE + q * (C_CH + 64) + (u * 64 + g * 16 + p * 4) * 2;
        st128<SC1>(ry, (u32)(cg * 16384 + u * 1024 + l * 16), __builtin_bit_cast(u32x4, cat8(tr_read(ad), tr_read(ad + 2048))));
      }
      if (w == 7 && l < 12) {
        f32x2 sum = {0.f, 0.f};
#pragma unroll
        for (int j = 0; j < 16; ++j) sum += lds_ld<f32x2>(sb + GP_BASE + (j * 24 + 2 * l) * 4);
        const int n = cg * 24 + 2 * l, which = n >> 10;
        u32 pk = pk2(sum[0], sum[1]);
        if (which == 1 || which == 4) pk = pk2(1.f + bflo(pk), 1.f + bfhi(pk));
        st32<SC1>(mkrsrc(ws + WS_MOD, 6144 * 2), (u32)(n * 2), pk);
      }
    }
    wait_vm<0>();
    asm volatile("" ::"v"(pf[0]), "v"(pf[1]), "v"(pf[2]), "v"(pf[3]));
    if constexpr (HI > 1) asm volatile("" : "+v"(xw[0]), "+v"(xw[1]));
    lds_bar();
    if (w == 0 && l < 8) flag_add(ws, FL_A2A + l * 8 + x);
    STAMP(1, 7);
  }

  if constexpr (LO <= 2 && HI > 2) {
    if (w >= 4) {
      L1.init(w - 4, l, sb, ws + WS_H2F, S_ * D_ * 2, pm * 32 * 8192, w1, D_ * F_ * 2, pn * 448 - (pn & 1) * 64);
      L1.prefetchB();
    }
  }

  if constexpr (LO <= 1 && HI > 1) {
    STAMP(2, 0);
    if constexpr (LO == 1) {
#pragma unroll
      for (int j = 0; j < 2; ++j) xw[j] = ld128<AUX_IN>(mkrsrc(xin, S_ * D_ * 2), (u32)(srow * 2048 + (cj0 + j * 8) * 16));
    }
    float xv[16], y[16], mean, rstd;
    unpack8(xw[0], xv); unpack8(xw[1], xv + 8);
    row_stats(xv, sb + LN_ST, w, l, mean, rstd);
    STAMP(2, 1);
    if (w == 0) flag_wait(ws, (FL_A2A + x * 8 + (l & 7)) * 128, 32);
    wg_bar();
    STAMP(2, 2);
    rsrc_t ry = mkrsrc(ws + WS_YB, S_ * D_ * 2), rm = mkrsrc(ws + WS_MOD, 6144 * 2);
    const u32x4 m0 = ld128<SC1>(rm, (u32)(t * 16));
    u32x4 m1 = {0, 0, 0, 0};
    if (w < 2) m1 = ld128<SC1>(rm, (u32)(8192 + t * 16));
    u32x2 yw[2][2];
#pragma unroll
    for (int j = 0; j < 2; ++j)
#pragma unroll
      for (int h = 0; h < 2; ++h) yw[j][h] = ld64<SC1>(ry, (u32)(((2 * (cj0 + j * 8) + h) * 2048 + srow) * 8));
    lds_st<u32x4>(sb + t * 16, m0);
    if (w < 2) lds_st<u32x4>(sb + LN_MOD4 + t * 16, m1);
    lds_bar();
    STAMP(2, 3);
#pragma unroll
    for (int j = 0; j < 2; ++j) unpack8(u32x4{yw[j][0][0], yw[j][0][1], yw[j][1][0], yw[j][1][1]}, y + 8 * j);
    u32 x1p[8];
    {
      const float nm = -mean * rstd;
#pragma unroll
      for (int j = 0; j < 2; ++j) {
        float sa[8], ca[8], ga[8], x1[8];
        const u32 ad = sb + (cj0 + j * 8) * 16;
        unpack8(lds_ld<u32x4>(ad), sa); unpack8(lds_ld<u32x4>(ad + 2048), ca); unpack8(lds_ld<u32x4>(ad + 4096), ga);
#pragma unroll
        for (int e = 0; e < 8; ++e) x1[e] = fmaf(ga[e] * fmaf(fmaf(xv[8 * j + e], rstd, nm), ca[e], sa[e]), y[8 * j + e], xv[8 * j + e]);
#pragma unroll
        for (int e = 0; e < 4; ++e) { const u32 pk = pk2(x1[2 * e], x1[2 * e + 1]); x1p[4 * j + e] = pk; xv[8 * j + 2 * e] = bflo(pk); xv[8 * j + 2 * e + 1] = bfhi(pk); }
      }
    }
    const u32 fo = (u32)(pm * 32 * 8192 + (pn >> 1) * 1024 + (srow & 15) * 16);
    rsrc_t r1 = mkrsrc(ws + WS_X1F, S_ * D_ * 2), r2 = mkrsrc(ws + WS_H2F, S_ * D_ * 2);
#pragma unroll
    for (int j = 0; j < 2; ++j) {
      const int cj = cj0 + j * 8;
      st128<SC1>(r1, fo + (cj >> 2) * 8192 + (cj & 3) * 256, u32x4{x1p[4 * j], x1p[4 * j + 1], x1p[4 * j + 2], x1p[4 * j + 3]});
    }
    row_stats(xv, sb + LN_ST + 512, w, l, mean, rstd);
    {
      const float nm = -mean * rstd;
#pragma unroll
      for (int j = 0; j < 2; ++j) {
        const int cj = cj0 + j * 8;
        float sv[8], cv[8], h[8];
        unpack8(lds_ld<u32x4>(sb + 6144 + cj * 16), sv); unpack8(lds_ld<u32x4>(sb + LN_MOD4 + cj * 16), cv);
#pragma unroll
        for (int e = 0; e < 8; ++e) h[e] = fmaf(fmaf(xv[8 * j + e], rstd, nm), cv[e], sv[e]);
        st128<SC1>(r2, fo + (cj >> 2) * 8192 + (cj & 3) * 256, u32x4{pk2(h[0], h[1]), pk2(h[2], h[3]), pk2(h[4], h[5]), pk2(h[6], h[7])});
      }
    }
    STAMP(2, 4);
    wait_vm<0>();
    lds_bar();
    if (w == 0 && l < 4) flag_add(ws, FL_LN + pm * 4 + l);
    STAMP(2, 7);
  }

  if (w >= 4) {
    if constexpr (LO <= 2 && HI > 2) {
      STAMP(3, 0);
      flag_wait(ws, (FL_LN + pm * 4 + (x >> 1)) * 128, 16);
      STAMP(3, 3);
      L1.run();
      wg_bar();
      STAMP(3, 7);
    }
    if constexpr (HI > 3) {
      STAMP(4, 0);
      L2.init(w - 4, l, sb, ws + WS_HF, S_ * F_ * 2, (pm * 112 + kh * 56) * 8192, w2, F_ * D_ * 2, kh * 1792 * 2048 + (pn & 7) * 256);
      L2.prefetchB();
      flag_wait(ws, (FL_G1 + (pm * 2 + kh) * 2 + ((x >> 1) & 1)) * 128, 32);
      STAMP(4, 3);
      L2.run();
      wg_bar();
      STAMP(4, 7);
    }
    return;
  }
  const int wm = w >> 1, wc = w & 1, g = l >> 4;
  if constexpr (LO <= 2 && HI > 2) {
    STAMP(3, 0);
    Consumer<G1> c;
    c.run(wm, wc, l, sb, (pn & 1) * 2, ws, 3);
    STAMP(3, 2);
    rsrc_t rh = mkrsrc(ws + WS_HF, S_ * F_ * 2);
    const u32 hb = (u32)(((pm * 112 + pn * 7) * 8 + wm * 4) * 1024 + l * 16);
#pragma unroll
    for (int m = 0; m < 4; ++m) {
      u32x2 o[7];
#pragma unroll
      for (int j = 0; j < 7; ++j) { const f32x4 a = c.acc[m][j]; o[j] = u32x2{silu_pk(a[0], a[1]), silu_pk(a[2], a[3])}; }
      if (wc == 0) {
#pragma unroll
        for (int j = 0; j < 3; ++j) st128<SC1>(rh, hb + j * 8192 + m * 1024, u32x4{o[2 * j][0], o[2 * j][1], o[2 * j + 1][0], o[2 * j + 1][1]});
        st64<SC1>(rh, hb + 3 * 8192 + m * 1024, o[6]);
      } else {
        st64<SC1>(rh, hb + 3 * 8192 + m * 1024 + 8, o[0]);
#pragma unroll
        for (int j = 0; j < 3; ++j) st128<SC1>(rh, hb + (4 + j) * 8192 + m * 1024, u32x4{o[2 * j + 1][0], o[2 * j + 1][1], o[2 * j + 2][0], o[2 * j + 2][1]});
      }
    }
    STAMP(3, 4);
    wait_vm<0>();
    if (l < 2) flag_add(ws, FL_G1 + (pm * 2 + kh) * 2 + l);
    STAMP(3, 7);
  }
  if constexpr (HI > 3) {
    STAMP(4, 0);
    Consumer<G2> c;
    c.run(wm, wc, l, sb, 0, ws, 4);
    STAMP(4, 2);
    rsrc_t rx = mkrsrc(ws + WS_XCH, 256 * 32768);
    f32x4 keep[4][2];
#pragma unroll
    for (int m = 0; m < 4; ++m)
#pragma unroll
      for (int j = 0; j < 2; ++j) {
        keep[m][j] = kh ? c.acc[m][2 + j] : c.acc[m][j];
        const f32x4 away = kh ? c.acc[m][j] : c.acc[m][2 + j];
        st128<SC1>(rx, (u32)(b * 32768 + w * 8192 + (m * 2 + j) * 1024 + l * 16), __builtin_bit_cast(u32x4, away));
      }
    u32x2 gbp[2], xr[4][2];
    rsrc_t rm = mkrsrc(ws + WS_MOD, 6144 * 2), r1 = mkrsrc(ws + WS_X1F, S_ * D_ * 2);
    const int jt0 = (pn & 7) * 8 + wc * 4 + kh * 2;
#pragma unroll
    for (int j = 0; j < 2; ++j) {
      gbp[j] = ld64<SC1>(rm, (u32)((5 * 1024 + (jt0 + j) * 16 + 4 * g) * 2));
#pragma unroll
      for (int m = 0; m < 4; ++m)
        xr[m][j] = ld64<SC1>(r1, (u32)(((pm * 32 + (jt0 >> 1)) * 8 + wm * 4 + m) * 1024 + ((2 * j + (g >> 1)) * 16 + (l & 15)) * 16 + (g & 1) * 8));
    }
    wait_vm<0>();
    if (l == 0) __hip_atomic_store((GAS u32*)(ws + WS_FLG + (size_t)(FL_XC + b) * 128 + w * 4), 1u, __ATOMIC_RELAXED, __HIP_MEMORY_SCOPE_AGENT);
    STAMP(4, 4);
    flag_wait(ws, (FL_XC + (b ^ 4)) * 128 + w * 4, 1);
    STAMP(4, 5);
    f32x4 oth[4][2];
#pragma unroll
    for (int m = 0; m < 4; ++m)
#pragma unroll
      for (int j = 0; j < 2; ++j) oth[m][j] = __builtin_bit_cast(f32x4, ld128<SC1>(rx, (u32)((b ^ 4) * 32768 + w * 8192 + (m * 2 + j) * 1024 + l * 16)));
#pragma unroll
    for (int m = 0; m < 4; ++m) {
      u32x2 r[2];
#pragma unroll
      for (int j = 0; j < 2; ++j) {
        const f32x4 a = keep[m][j] + oth[m][j]; const u32 x0 = xr[m][j][0], x1 = xr[m][j][1], g0 = gbp[j][0], g1 = gbp[j][1];
        r[j] = u32x2{pk2(fmaf(bflo(g0), a[0], bflo(x0)), fmaf(bfhi(g0), a[1], bfhi(x0))), pk2(fmaf(bflo(g1), a[2], bflo(x1)), fmaf(bfhi(g1), a[3], bfhi(x1)))};
      }
      const u32 p0 = r[0][0], p1 = r[0][1], q0 = r[1][0], q1 = r[1][1];
      auto s0 = __builtin_amdgcn_permlane16_swap(p0, q0, false, false);
      auto s1 = __builtin_amdgcn_permlane16_swap(p1, q1, false, false);
      const u32 e0 = s0[0], f0 = s0[1], e1 = s1[0], f1 = s1[1];
      *(u32x4*)(out + (size_t)(pm * 128 + wm * 64 + m * 16 + (l & 15)) * 1024 + (jt0 + (g & 1)) * 16 + 8 * (g >> 1)) = u32x4{e0, e1, f0, f1};
    }
    STAMP(4, 7);
  }
}

template <int LO, int HI> static void launch_kp(void* const* d_in, char* ws, void* d_out, hipStream_t stream) {
  (void)hipFuncSetAttribute((const void*)kp<LO, HI>, hipFuncAttributeMaxDynamicSharedMemorySize, KP_LDS);
  kp<LO, HI><<<256, 512, KP_LDS, stream>>>(d_in[0], d_in[4], d_in[5], d_in[9], d_in[10], d_in[7], d_in[8], ws, (unsigned short*)d_out);
}

extern "C" void kernel_launch(void* const* d_in, const int* in_sizes, int n_in, void* d_out, int out_size, void* d_ws, size_t ws_size,
                              hipStream_t stream) {
  char* ws = (char*)d_ws;
  k1_prep<<<256, 256, 0, stream>>>(d_in[1], d_in[2], d_in[3], d_in[4], ws);
  launch_kp<0, 4>(d_in, ws, d_out, stream);
}
```

```cpp
#include <hip/hip_runtime.h>
#include <hip/hip_bf16.h>

typedef unsigned u32;
typedef unsigned u32x2 __attribute__((ext_vector_type(2)));
typedef unsigned u32x4 __attribute__((ext_vector_type(4)));
typedef float f32x2 __attribute__((ext_vector_type(2)));
typedef float f32x4 __attribute__((ext_vector_type(4)));
typedef __bf16 bf16x2 __attribute__((ext_vector_type(2)));
typedef __bf16 bf16x4 __attribute__((ext_vector_type(4)));
typedef __bf16 bf16x8 __attribute__((ext_vector_type(8)));
#define LAS __attribute__((address_space(3)))
#define GAS __attribute__((address_space(1)))
#define DI __device__ __forceinline__
typedef __amdgpu_buffer_rsrc_t rsrc_t;

constexpr int S_ = 2048, D_ = 1024, F_ = 3584;
#define KP_CODE_BYTES 33280
constexpr int SC1 = 16;

constexpr size_t WS_KVT = 0;
constexpr size_t WS_YB = 4u << 20;
constexpr size_t WS_X1F = 8u << 20;
constexpr size_t WS_H2F = 12u << 20;
constexpr size_t WS_HF = 16u << 20;
constexpr size_t WS_XCH = 32u << 20;
constexpr size_t WS_MOD = 36u << 20;
constexpr size_t WS_FLG = WS_MOD + (64u << 10);
constexpr size_t WS_MODP = WS_MOD + (256u << 10);
constexpr int FL_A2A = 0, FL_LN = 64, FL_MOD = 128, FL_XC = 192, FL_G1 = 192 + 256, FL_LINES = FL_G1 + 3 * 64;

extern __shared__ __attribute__((aligned(16))) char dsm[];

DI rsrc_t mkrsrc(const void* p, u32 bytes) { return __builtin_amdgcn_make_buffer_rsrc((void*)p, 0, bytes, 0x00020000); }
DI float bflo(u32 w) { return __uint_as_float(w << 16); }
DI float bfhi(u32 w) { return __uint_as_float(w & 0xffff0000u); }
DI u32 pk2(float a, float b) { f32x2 v = {a, b}; bf16x2 r = __builtin_convertvector(v, bf16x2); return __builtin_bit_cast(u32, r); }
DI float silu_f(float x) { return x * __builtin_amdgcn_rcpf(1.f + __builtin_amdgcn_exp2f(x * -1.44269504089f)); }
DI u32 silu_pk(float a, float b) {
  return pk2(a * __builtin_amdgcn_rcpf(1.f + __builtin_amdgcn_exp2f(a)), b * __builtin_amdgcn_rcpf(1.f + __builtin_amdgcn_exp2f(b)));
}
template <int AUX> DI u32x4 ld128(rsrc_t r, u32 off) { return __builtin_amdgcn_raw_buffer_load_b128(r, off, 0, AUX); }
template <int AUX> DI u32x2 ld64(rsrc_t r, u32 off) { return __builtin_amdgcn_raw_buffer_load_b64(r, off, 0, AUX); }
template <int AUX> DI void st128(rsrc_t r, u32 off, u32x4 v) { __builtin_amdgcn_raw_buffer_store_b128(v, r, off, 0, AUX); }
template <int AUX> DI void st64(rsrc_t r, u32 off, u32x2 v) { __builtin_amdgcn_raw_buffer_store_b64(v, r, off, 0, AUX); }
DI bf16x4 tr_read(u32 a) { return __builtin_amdgcn_ds_read_tr16_b64_v4bf16((LAS bf16x4*)(size_t)a); }
DI bf16x8 cat8(bf16x4 a, bf16x4 b) { return __builtin_shufflevector(a, b, 0, 1, 2, 3, 4, 5, 6, 7); }
template <class T> DI auto lds_ld(u32 a) { return *(LAS T*)(size_t)a; }
template <class T, class V> DI void lds_st(u32 a, V v) { *(LAS T*)(size_t)a = v; }
DI void unpack8(u32x4 w, float* f) {
#pragma unroll
  for (int i = 0; i < 4; ++i) { u32 x = w[i]; f[2 * i] = bflo(x); f[2 * i + 1] = bfhi(x); }
}
template <int N> DI void wait_vm() { asm volatile("s_waitcnt vmcnt(%0)" ::"n"(N) : "memory"); }
DI void wg_bar() { asm volatile("s_barrier" ::: "memory"); }
DI void lds_bar() { asm volatile("s_waitcnt lgkmcnt(0)\n\ts_barrier" ::: "memory"); }
template <int CTRL> DI float dpp_add(float x) {
  return x + __int_as_float(__builtin_amdgcn_update_dpp(0, __float_as_int(x), CTRL, 0xf, 0xf, true));
}
DI float add_x16(float x) { const u32 u = __float_as_uint(x); auto r = __builtin_amdgcn_permlane16_swap(u, u, false, false); const u32 a = r[0], b = r[1]; return __uint_as_float(a) + __uint_as_float(b); }
DI float add_x32(float x) { const u32 u = __float_as_uint(x); auto r = __builtin_amdgcn_permlane32_swap(u, u, false, false); const u32 a = r[0], b = r[1]; return __uint_as_float(a) + __uint_as_float(b); }

DI void lds_meet(u32 addr, int l) {
  asm volatile("s_waitcnt lgkmcnt(0)" ::: "memory");
  LAS u32* cnt = (LAS u32*)(size_t)addr;
  if (l == 0) __hip_atomic_fetch_add(cnt, 1u, __ATOMIC_RELAXED, __HIP_MEMORY_SCOPE_WORKGROUP);
  while ((u32)__builtin_amdgcn_readfirstlane(__hip_atomic_load(cnt, __ATOMIC_RELAXED, __HIP_MEMORY_SCOPE_WORKGROUP)) < 4u) __builtin_amdgcn_s_sleep(1);
  asm volatile("" ::: "memory");
}
DI void flag_add(char* ws, int line) { __hip_atomic_fetch_add((GAS u32*)(ws + WS_FLG + (size_t)line * 128), 1u, __ATOMIC_RELAXED, __HIP_MEMORY_SCOPE_AGENT); }
DI void flag_wait(char* ws, u32 byteoff, u32 target) {
  const GAS u32* p = (const GAS u32*)(ws + WS_FLG + byteoff);
#pragma unroll 1
  for (u32 spins = 0; spins < (1u << 18); ++spins) {
    const u32 v = __hip_atomic_load(p, __ATOMIC_RELAXED, __HIP_MEMORY_SCOPE_AGENT);
    if (__all(v >= target)) break;
    __builtin_amdgcn_s_sleep(1);
  }
  __builtin_amdgcn_fence(__ATOMIC_ACQUIRE, "wavefront");
  asm volatile("" ::: "memory");
}
DI u32 touch(rsrc_t r, u32 voff) { return __builtin_amdgcn_raw_buffer_load_b32(r, voff, 0, 0); }

template <int AUX> DI void dma16(u32 voff, rsrc_t r, u32 soff, u32 ldsaddr) {
  if constexpr (AUX == SC1) asm volatile("s_mov_b32 m0, %2\n\ts_nop 0\n\tbuffer_load_dwordx4 %0, %1, %3 offen sc1 lds" ::"v"(voff), "s"(r), "s"(ldsaddr), "s"(soff) : "memory");
  else asm volatile("s_mov_b32 m0, %2\n\ts_nop 0\n\tbuffer_load_dwordx4 %0, %1, %3 offen lds" ::"v"(voff), "s"(r), "s"(ldsaddr), "s"(soff) : "memory");
}

DI float red8(float x) { return add_x32(add_x16(dpp_add<0x128>(x))); }

constexpr int K1_GV = 64 * 320, K1_SC = K1_GV + 49152, K1_CNT = K1_SC + 256, K1_LDS = K1_CNT + 16;
__global__ __launch_bounds__(512) void k1_prep(const void* __restrict__ k, const void* __restrict__ v, const void* __restrict__ shk,
                                               const void* __restrict__ ssmk, const void* __restrict__ cin, const void* __restrict__ wmod, char* __restrict__ ws) {
  const u32 sb = (u32)(size_t)(LAS char*)dsm;
  const int t = threadIdx.x, b = blockIdx.x;
  if (__builtin_amdgcn_readfirstlane(t) >= 256) {
    const int ta = t - 256, aw = __builtin_amdgcn_readfirstlane(ta >> 6), l = t & 63, idx = b >> 3;
    const int r0 = (idx >> 2) * 128, col0 = (b & 7) * 768 + (idx & 3) * 192;
    rsrc_t rw = mkrsrc(wmod, 1024 * 6144 * 2);
    const u32 cw = __builtin_amdgcn_raw_buffer_load_b16(mkrsrc(cin, 1024 * 2), (u32)((r0 + (ta & 127)) * 2), 0, 0);
    wg_bar();
#pragma unroll
    for (int i = 0; i < 3; ++i) {
      const int o = i * 1024 + l * 16, row = o / 384, cb = o % 384;
      const u32 vo = (u32)(row * 12288 + (cb ^ (((row >> 1) & 3) << 5)));
#pragma unroll
      for (int j = 0; j < 4; ++j) dma16<0>(vo, rw, (u32)((r0 + aw * 32 + j * 8) * 12288 + col0 * 2), sb + K1_GV + aw * 12288 + j * 3072 + i * 1024);
    }
    asm volatile("" ::: "memory");
    const u32 pf = touch(mkrsrc(ssmk, D_ * S_ * 2), (u32)(((b & 7) * 32 + idx) * 16384 + (ta & 127) * 128));
    u32 lo, hi;
    asm volatile("s_getpc_b64 s[20:21]\n\ts_add_u32 s20, s20, kp_main@rel32@lo+4\n\ts_addc_u32 s21, s21, kp_main@rel32@hi+12\n\ts_mov_b32 %0, s20\n\ts_mov_b32 %1, s21"
                 : "=s"(lo), "=s"(hi) : : "s20", "s21", "scc");
    const u32 pc = touch(mkrsrc((const void*)(((unsigned long long)hi << 32) | lo), KP_CODE_BYTES), (u32)((idx * 16 + (t & 15)) * 128));
    wait_vm<2>();
    lds_st<unsigned short>(sb + K1_SC + (ta & 127) * 2, (unsigned short)pk2(silu_f(bflo(cw)), 0.f));
    lds_meet(sb + K1_CNT + 4, l);
    const int g = l >> 4, row = 4 * g + ((l & 15) >> 2);
    f32x4 d[3] = {{0, 0, 0, 0}, {0, 0, 0, 0}, {0, 0, 0, 0}};
#pragma unroll
    for (int ks = 0; ks < 4; ++ks) {
      const bf16x8 sc = cat8(lds_ld<bf16x4>(sb + K1_SC + ks * 64 + g * 8), lds_ld<bf16x4>(sb + K1_SC + ks * 64 + 32 + g * 8));
#pragma unroll
      for (int j = 0; j < 3; ++j) {
        const u32 ad = sb + K1_GV + ks * 12288 + row * 384 + (((aw * 3 + j) ^ ((row >> 1) & 3)) << 5) + (l & 3) * 8;
        d[j] = __builtin_amdgcn_mfma_f32_16x16x32_bf16(sc, cat8(tr_read(ad), tr_read(ad + 16 * 384)), d[j], 0, 0, 0);
      }
    }
    if (g < 3) ((float*)(ws + WS_MODP))[(idx >> 2) * 6144 + col0 + aw * 48 + l] = g == 0 ? d[0][0] : g == 1 ? d[1][0] : d[2][0];
    asm volatile("" ::"v"(pf), "v"(pc));
    return;
  }
  const int dblk = b & 7, sblk = b >> 3;
  const int a = t >> 4, cc = t & 15;
  const int s0 = sblk * 64 + a * 4, ch0 = dblk * 128 + cc * 8;
  rsrc_t rk = mkrsrc(k, S_ * D_ * 2), rv = mkrsrc(v, S_ * D_ * 2), rs = mkrsrc(shk, D_ * 4 * 2);
  u32x4 kk[7], vv[4], sh[4];
#pragma unroll
  for (int j = 0; j < 7; ++j) kk[j] = ld128<0>(rk, (u32)((s0 - 3 + j) * 2048 + ch0 * 2));
#pragma unroll
  for (int j = 0; j < 4; ++j) vv[j] = ld128<0>(rv, (u32)((s0 + j) * 2048 + ch0 * 2));
#pragma unroll
  for (int j = 0; j < 4; ++j) sh[j] = ld128<0>(rs, (u32)(ch0 * 8 + j * 16));
  if (t == 0) lds_st<u32x2>(sb + K1_CNT, u32x2{0u, 0u});
  __builtin_amdgcn_sched_barrier(0);
  lds_bar();
  __builtin_amdgcn_sched_barrier(0);
  float shf[8][4];
#pragma unroll
  for (int j = 0; j < 4; ++j) { float f[8]; unpack8(sh[j], f);
#pragma unroll
    for (int i = 0; i < 8; ++i) shf[2 * j + (i >> 2)][i & 3] = f[i]; }
  float kf[7][8];
#pragma unroll
  for (int j = 0; j < 7; ++j) unpack8(kk[j], kf[j]);
#pragma unroll
  for (int r = 0; r < 4; ++r) {
    float vf[8], o[8]; unpack8(vv[r], vf);
#pragma unroll
    for (int e = 0; e < 8; ++e) {
      float x = kf[r][e] * shf[e][3];
      x = fmaf(kf[r + 1][e], shf[e][2], x);
      x = fmaf(kf[r + 2][e], shf[e][1], x);
      x = fmaf(kf[r + 3][e], shf[e][0], x);
      o[e] = x * vf[e];
    }
    u32x4 w = {pk2(o[0], o[1]), pk2(o[2], o[3]), pk2(o[4], o[5]), pk2(o[6], o[7])};
    lds_st<u32x4>(sb + (a * 4 + r) * 320 + cc * 16, w);
  }
  const int w = t >> 6, l = t & 63, g = l >> 4, q = (l & 15) >> 2, p = l & 3;
  lds_meet(sb + K1_CNT, l);
  rsrc_t rd = mkrsrc(ws + WS_KVT, S_ * D_ * 2);
#pragma unroll
  for (int i = 0; i < 4; ++i) {
    const int u = w * 4 + i, s8 = u >> 1, hf = u & 1;
    const u32 ad = sb + (s8 * 8 + q) * 320 + (hf * 64 + g * 16 + p * 4) * 2;
    bf16x8 r = cat8(tr_read(ad), tr_read(ad + 4 * 320));
    st128<0>(rd, (u32)((((sblk * 4 + (s8 >> 1)) * 256 + dblk * 32 + hf * 16 + (l >> 2)) * 8 + (s8 & 1) * 4 + (l & 3)) * 16), __builtin_bit_cast(u32x4, r));
  }
  if (t < 3 && b * 3 + t < FL_LINES) *(u32x4*)(ws + WS_FLG + (size_t)(b * 3 + t) * 128) = u32x4{0, 0, 0, 0};
}

template <int MODE_, int NT_, int KS_, int NS_, int LDB_, int PB_>
struct GC {
  static constexpr int MODE = MODE_, NT = NT_, KS = KS_, NS = NS_, SY = 2, LDB = LDB_;
  static constexpr int PB = PB_;
  static constexpr int OWN = MODE == 1 ? 0 : 7;
  static constexpr int NPB = MODE == 1 ? 4 : 2;
  static constexpr int PSTR = MODE == 1 ? 1088 : 1024, SB = 8192 + 4 * NPB * PSTR, NB = KS / SY, LDS = NS * SB;
  static constexpr int mn(int a, int b) { return a < b ? a : b; }
  static constexpr int need(int j) { return mn(SY * (j + 1), KS - 1); }
  static constexpr int issued(int j) { return j <= 0 ? NS - 1 : mn(SY * (j - 1) + NS, KS - 1); }
  static constexpr int cnt(int a, int b) { int n = 0; for (int s = a; s <= b; ++s) n += s < PB ? 2 : 2 + NPB; return n; }
  static constexpr int prod(int s) { return s < 21 ? (s - 7) >> 1 : s < 42 ? (s - 21) / 3 : (s - 42) >> 1; }
  static constexpr int kloc(int s) { return MODE == 1 || s < 7 ? s : s < 21 ? 4 * ((s - 7) & 1) : s < 42 ? 1 + 2 * ((s - 21) % 3) : 2 + 4 * (s & 1); }
  static constexpr int gate(int j) { return MODE == 1 || j < 0 ? 0 : (issued(j) < 21 && issued(j + 1) >= 21) ? 1 : (issued(j) < 42 && issued(j + 1) >= 42) ? 2 : 0; }
  static constexpr int slot(int s) { return MODE == 2 && s == NS ? NS : s % NS; }
  static constexpr int PD = (NS + SY) / SY - 2;
};

template <class C> struct Loader {
  u32 voffA, voffB; rsrc_t rA, rB, rF; u32 soffA, soffB, ldsA, ldsB, ldsF, flag; int own; char* ws;
  template <int ST> DI u32 kstep() {
    if constexpr (C::MODE == 1) return ST;
    else if constexpr (ST < C::OWN) return own * 7 + ST;
    else return (C::prod(ST) >= own ? 7 : 0) + C::prod(ST) * 7 + C::kloc(ST);
  }
  template <int ST> DI void stA() {
    const u32 so = soffA + kstep<ST>() * 8192;
#pragma unroll
    for (int i = 0; i < 2; ++i) dma16<SC1>(voffA, rA, so + i * 1024, ldsA + C::slot(ST) * C::SB + i * 1024);
  }
  template <int ST> DI void stB() {
    const u32 so = soffB + kstep<ST>() * (32 * C::LDB);
#pragma unroll
    for (int i = 0; i < C::NPB; ++i) {
      if constexpr (C::MODE == 1) dma16<0>(voffB, rB, so + 2 * i * C::LDB, ldsB + C::slot(ST) * C::SB + ((i & 1) + 4 * (i >> 1)) * C::PSTR);
      else dma16<0>(voffB ^ (i * 128), rB, so + 4 * i * C::LDB, ldsB + C::slot(ST) * C::SB + i * 1024);
    }
  }
  template <int A, int B, int WHAT> DI void stages() { if constexpr (A <= B) { if constexpr (WHAT & 1) stA<A>(); if constexpr (WHAT & 2) stB<A>(); stages<A + 1, B, WHAT>(); } }
  template <int J> DI void step() {
    if constexpr (J < C::NB) {
      wait_vm<C::cnt(C::need(J) + 1, C::issued(J)) + (C::MODE == 1 && C::issued(J) == C::KS - 1 ? 7 : 0)>();
      wg_bar();
      if constexpr (C::gate(J + C::PD) != 0) {
        static_assert(C::issued(J) + 1 <= C::need(J + C::PD), "the poll must be older than the stage the later wait covers");
        asm volatile("s_mov_b32 m0, %2\n\ts_nop 0\n\tbuffer_load_dword %0, %1, %3 offen sc1 lds" ::"v"(0u), "s"(rF), "s"(ldsF), "s"(flag + C::gate(J + C::PD) * 64 * 128) : "memory");
      }
      if constexpr (C::gate(J) != 0) {
        if ((u32)__builtin_amdgcn_readfirstlane(lds_ld<volatile u32>(ldsF)) < 32u) flag_wait(ws, flag + C::gate(J) * 64 * 128, 32);
      }
      stages<C::issued(J) + 1, C::issued(J + 1), 3>();
      if constexpr (C::MODE == 1 && C::issued(J) < C::KS - 1 && C::issued(J + 1) == C::KS - 1) {
#pragma unroll
        for (int i = 0; i < 7; ++i) dma16<0>(voffA, rF, flag + i * 1024, ldsF);
      }
      step<J + 1>();
    }
  }
  DI void init(int lw, int l, u32 sb, const void* Aptr, u32 Abytes, u32 aoff, const void* Bptr, u32 Bbytes, u32 boff) {
    rA = mkrsrc(Aptr, Abytes); rB = mkrsrc(Bptr, Bbytes);
    voffA = l * 16; soffA = aoff + lw * 2048; ldsA = sb + lw * 2048;
    soffB = boff + lw * 8 * C::LDB;
    if constexpr (C::MODE == 1) {
      const int h = l >> 5;
      voffB = h * C::LDB + (((l & 31) * 16) ^ (h * 32));
      ldsB = sb + 8192 + (2 * (lw & 1) + 8 * (lw >> 1)) * C::PSTR;
    } else {
      const int h = l >> 4;
      voffB = h * C::LDB + (((l & 15) * 16) ^ (h * 32));
      ldsB = sb + 8192 + lw * 2048;
    }
  }
  DI void prefetchB() { stages<0, C::PB - 1, 2>(); }
  DI void run1() {
    stages<0, 1, 1>();
    wait_vm<2>();
    wg_bar();
    stages<2, C::PB - 1, 1>();
    stages<C::PB, C::NS - 1, 3>();
    step<0>();
  }
  DI void run2() {
    static_assert(C::PB == C::NS + 1 && C::OWN == C::NS - 1 && C::gate(2 + C::PD) == 0, "start-up");
    wait_vm<0>();
    wg_bar();
    flag_wait(ws, flag, 32);
    stages<C::OWN, C::NS, 1>();
    wg_bar();
    stages<C::NS + 1, C::issued(3), 3>();
    step<3>();
  }
};

template <class C> struct Consumer {
  static constexpr int NT = C::NT, NBASE = C::MODE == 1 ? 3 : NT, NR = 4 + 2 * NT, NM = 4 * NT, PRE = C::MODE == 1 ? 2 : 4;
  static constexpr int TOFF = C::MODE == 1 ? 4 * C::PSTR : 4096, WIN = C::NS / 2;
  static_assert(PRE + NR <= NM && C::NS % 2 == 0 && (WIN - 1) * C::SB + TOFF + 4096 < 65536, "slots");
  f32x4 acc[4][NT];
  u32x4 Af[2][4]; u32x2 Bl[2][NT], Bh[2][NT];
  u32 ac[3], bc[3][NBASE];
  template <int X, int SL, int R> DI void rd1() {
    constexpr int wi = SL / WIN, so = (SL % WIN) * C::SB;
    if constexpr (R >= 2 && R < 6) asm volatile("ds_read_b128 %0, %1 offset:%2" : "=v"(Af[X][R - 2]) : "v"(ac[wi]), "i"(so + (R - 2) * 1024) : "memory");
    else if constexpr (R < NR) {
      constexpr int q = R < 2 ? R : R - 4, j = q >> 1, h = q & 1, bi = C::MODE == 1 ? (j == 6 ? 2 : (j & 1)) : j, off = so + (C::MODE == 1 && j < 6 ? 32 * j : 0) + h * TOFF;
      if constexpr (h == 0) asm volatile("ds_read_b64_tr_b16 %0, %1 offset:%2" : "=v"(Bl[X][j]) : "v"(bc[wi][bi]), "i"(off) : "memory");
      else asm volatile("ds_read_b64_tr_b16 %0, %1 offset:%2" : "=v"(Bh[X][j]) : "v"(bc[wi][bi]), "i"(off) : "memory");
    }
  }
  template <int X, int K> DI void mm1() {
    constexpr int j = K >> 2, m = (j & 1) ? 3 - (K & 3) : (K & 3);
    const u32x4 b = {Bl[X][j][0], Bl[X][j][1], Bh[X][j][0], Bh[X][j][1]};
    asm volatile("v_mfma_f32_16x16x32_bf16 %0, %1, %2, %0" : "+v"(acc[m][j]) : "v"(b), "v"(Af[X][m]));
  }
  static constexpr int lastrd(int K) { int r = -1; for (int k = 0; k <= K; ++k) { const int j = k >> 2, m = (j & 1) ? 3 - (k & 3) : (k & 3), rb = j == 0 ? 1 : 2 * j + 5, ra = 2 + m; r = rb > r ? rb : r; r = ra > r ? ra : r; } return r; }
  template <int N> DI void wl() { __builtin_amdgcn_s_waitcnt(0xC07F | ((N > 15 ? 15 : N) << 8)); }
  template <int ST, int K, bool BAR, bool RD, int SL = (ST + 1) % C::NS> DI void stepk() {
    if constexpr (K < NM) {
      if constexpr (K == 0 && PRE > 0) wl<NR - 1 - lastrd(PRE - 1)>();
      if constexpr (K == PRE) { wl<0>(); if constexpr (BAR) wg_bar(); }
      mm1<ST & 1, K>();
      if constexpr (RD && K >= PRE) rd1<(ST & 1) ^ 1, SL, K - PRE>();
      stepk<ST, K + 1, BAR, RD, SL>();
    }
  }
  template <int A, int B, bool BAR = true> DI void steps() { if constexpr (A < B) { stepk<A, 0, BAR && !(A & 1), true>(); steps<A + 1, B, BAR>(); } }
  template <int R> DI void rdall() { if constexpr (R < NR) { rd1<0, 0, R>(); rdall<R + 1>(); } }
  DI void init(int wm, int wc, int l, u32 sb, int coff) {
    const int g = l >> 4, q = (l & 15) >> 2, p = l & 3;
    ac[0] = sb + wm * 4096 + l * 16;
    if constexpr (C::MODE == 1) {
      const int base = sb + 8192 + ((q >> 1) + 2 * (g & 1) + 8 * (g >> 1)) * C::PSTR + (q & 1) * 512 + 8 * p + 32 * (8 * wc + coff);
      const int dl = (q & 1) * 32;
      bc[0][0] = base + dl;
      bc[0][1] = base - dl;
      bc[0][2] = wc ? base - 32 - dl : base + 192 + dl;
    } else {
      const int row = 4 * g + q;
#pragma unroll
      for (int j = 0; j < NT; ++j) bc[0][j] = sb + 8192 + row * 256 + 8 * p + 32 * ((NT * wc + (j ^ coff)) ^ (row & 7));
    }
    ac[1] = ac[0] + WIN * C::SB;
    asm volatile("" : "+v"(ac[0]), "+v"(ac[1]));
#pragma unroll
    for (int i = 0; i < NBASE; ++i) { bc[1][i] = bc[0][i] + WIN * C::SB; asm volatile("" : "+v"(bc[0][i]), "+v"(bc[1][i])); }
#pragma unroll
    for (int m = 0; m < 4; ++m)
#pragma unroll
      for (int j = 0; j < NT; ++j) { acc[m][j] = f32x4{0, 0, 0, 0}; asm volatile("" : "+v"(acc[m][j])); }
  }
  DI void spare() {
    ac[2] = ac[0] + C::LDS; asm volatile("" : "+v"(ac[2]));
#pragma unroll
    for (int i = 0; i < NBASE; ++i) { bc[2][i] = bc[0][i] + C::LDS; asm volatile("" : "+v"(bc[2][i])); }
  }
  template <int N> DI void rounds() {
#pragma unroll 1
    for (int j = 0; j < N; ++j) steps<0, C::NS>();
  }
  DI void done() {
    asm volatile("s_waitcnt lgkmcnt(0)\n\ts_nop 7\n\ts_nop 1" ::: "memory");
#pragma unroll
    for (int m = 0; m < 4; ++m)
#pragma unroll
      for (int j = 0; j < NT; ++j) asm volatile("" : "+v"(acc[m][j]));
#pragma unroll
    for (int m = 0; m < 4; ++m) asm volatile("" ::"v"(Af[0][m]), "v"(Bl[0][m]), "v"(Bh[0][m]));
  }
};
typedef GC<1, 7, 32, 6, F_ * 2, 2> G1;
typedef GC<2, 4, 56, 8, D_ * 2, 9> G2;

constexpr int U_BASE = 128, U_STR = 4576, C_STR = 4160, C_CH = 4 * C_STR, C_BASE = U_BASE + 4 * U_STR, GP_BASE = C_BASE + 4 * C_CH + 256;
constexpr int LN_MOD = 4 * G1::SB, LN_MOD4 = 5 * G1::SB, LN_ST = LN_MOD4 + 2048;
static_assert(LN_MOD >= GP_BASE + 256 && G1::NS >= 6, "adaLN scratch");
constexpr int XH_BASE = G2::LDS + G2::SB + 1024;
constexpr int KP_LDS = G1::LDS + 1024;
static_assert(KP_LDS >= XH_BASE + 4096, "LDS");
static_assert(KP_LDS <= 163840, "LDS");

struct Conv {
  f32x4 acc[8]; u32x2 Al[2][8], Ah[2][8]; u32x4 B[2][8]; u32 aA, aB;
  static constexpr int seq(int k, bool tile) {
    int c = 0;
    for (int r = 0; r < 8; ++r) {
      const int n[5] = {7, r < 7 ? 6 : 0, r < 6 ? 5 : 1, r < 5 ? 4 : 2, 3}, i[5] = {r, r < 7 ? r : 0, r < 6 ? r : r - 6, r < 5 ? r : r - 5, r >> 1};
      for (int e = 0; e < ((r & 1) ? 4 : 5); ++e) { if (c == k) return tile ? n[e] : i[e]; ++c; }
    }
    return 0;
  }
  template <int X, int T0, int R> DI void rd1() {
    constexpr int i = R / 3, k = R % 3;
    if constexpr (k == 0) asm volatile("ds_read_b64_tr_b16 %0, %1 offset:%2" : "=v"(Al[X][i]) : "v"(aA), "i"(64 * (8 * i + T0) + 8) : "memory");
    if constexpr (k == 1) asm volatile("ds_read_b64_tr_b16 %0, %1 offset:%2" : "=v"(Ah[X][i]) : "v"(aA), "i"(64 * (8 * i + T0)) : "memory");
    if constexpr (k == 2) asm volatile("ds_read_b128 %0, %1 offset:%2" : "=v"(B[X][7 - i]) : "v"(aB), "i"(512 * (7 - i) + 64 * (2 - T0)) : "memory");
  }
  template <int X, int T0, int K> DI void stepk() {
    if constexpr (K < 36) {
      constexpr int n = seq(K, true), i = seq(K, false);
      const u32x4 a = {Al[X][i][0], Al[X][i][1], Ah[X][i][0], Ah[X][i][1]};
      asm volatile("v_mfma_f32_16x16x32_bf16 %0, %1, %2, %0" : "+v"(acc[n]) : "v"(a), "v"(B[X][n - i]));
      if constexpr (K < 24) rd1<X ^ 1, T0, K>();
      stepk<X, T0, K + 1>();
    }
  }
  template <int R> DI void rdall() { if constexpr (R < 24) { rd1<0, 0, R>(); rdall<R + 1>(); } }
};

DI void row_stats_a(const float (&x)[16], u32 slot, int w, int l) {
  float s = 0.f;
#pragma unroll
  for (int e = 0; e < 16; ++e) s += x[e];
  const float mw = red8(s) * (1.f / 128.f);
  float qv = 0.f;
#pragma unroll
  for (int e = 0; e < 16; ++e) { const float d = x[e] - mw; qv = fmaf(d, d, qv); }
  qv = red8(qv);
  if (l < 8) lds_st<f32x2>(slot + (l * 8 + w) * 8, f32x2{mw, qv});
}
DI void row_stats_b(u32 slot, int l, float& mean, float& rstd) {
  f32x4 p[4];
#pragma unroll
  for (int i = 0; i < 4; ++i) p[i] = lds_ld<f32x4>(slot + (l & 7) * 64 + i * 16);
  float m = 0.f;
#pragma unroll
  for (int i = 0; i < 4; ++i) m += p[i][0] + p[i][2];
  m *= 0.125f;
  float m2 = 0.f, dd = 0.f;
#pragma unroll
  for (int i = 0; i < 4; ++i) { m2 += p[i][1] + p[i][3]; const float d0 = p[i][0] - m, d1 = p[i][2] - m; dd = fmaf(d0, d0, dd); dd = fmaf(d1, d1, dd); }
  mean = m;
  rstd = __builtin_amdgcn_rsqf(fmaf(dd, 128.f, m2) * (1.f / 1024.f) + 1e-5f);
}

extern "C" __global__ __launch_bounds__(512, 2) void kp_main(const void* __restrict__ xin, const void* __restrict__ ssmk, const void* __restrict__ ssmd,
                                                             const void* __restrict__ w1, const void* __restrict__ w2, char* __restrict__ ws, void* __restrict__ out) {
  const u32 sb = (u32)(size_t)(LAS char*)dsm;
  const int t = threadIdx.x, b = blockIdx.x, w = __builtin_amdgcn_readfirstlane(t >> 6), l = t & 63;
  const int x = b & 7, idx = b >> 3, cg = x * 32 + idx;
  const int pm = (x & 1) * 8 + (idx & 7), pn = (x >> 1) * 4 + (idx >> 3), kh = x >> 2;
  const int srow = pm * 128 + pn * 8 + (l & 7), cj0 = w * 16 + (l >> 3);
  u32x4 xw[2];
#define LOAD_X _Pragma("unroll") for (int j = 0; j < 2; ++j) xw[j] = ld128<0>(mkrsrc(xin, S_ * D_ * 2), (u32)(srow * 2048 + (cj0 + j * 8) * 16));
  float xv[16];
  Loader<G1> L1; Loader<G2> L2;

  {
    if (w < 4) {
      const int ch = cg * 4 + w;
      rsrc_t rk = mkrsrc(ssmk, D_ * S_ * 2);
      u32x4 kw[4]; u32x2 khl[4];
#pragma unroll
      for (int q = 0; q < 4; ++q) {
        const u32 o = (u32)(ch * 4096 + (l + 64 * q) * 16);
        kw[q] = ld128<0>(rk, o);
        khl[q] = ld64<0>(rk, o - 8);
      }
      u32x4 uw[4];
#pragma unroll
      for (int q = 0; q < 4; ++q) uw[q] = ld128<0>(mkrsrc(ws + WS_KVT, S_ * D_ * 2), (u32)((((t >> 3) + 32 * q) * 256 + cg) * 128 + (t & 7) * 16));
      const float dval = bflo(((const unsigned short*)ssmd)[ch]);
      if (t == 0) lds_st<u32x4>(sb + GP_BASE, u32x4{0u, 0u, 0u, 0u});
      __builtin_amdgcn_sched_barrier(0);
      lds_bar();
      __builtin_amdgcn_sched_barrier(0);
      const u32x4 z4 = {0, 0, 0, 0};
      if (l < 30) lds_st<u32x4>(sb + U_BASE + w * U_STR + l * 16, z4);
      if (l < 8) lds_st<u32x4>(sb + C_BASE + w * C_CH + (l >> 1) * C_STR + (l & 1) * 16, z4);
      if (l == 0) khl[0] = u32x2{0, 0};
#pragma unroll
      for (int q = 0; q < 4; ++q) {
        const u32 wm2 = khl[q][0], wm1 = khl[q][1], w0 = kw[q][0], w1_ = kw[q][1], w2_ = kw[q][2], w3 = kw[q][3];
        const u32 am = __builtin_amdgcn_alignbit(wm1, wm2, 16), a0 = __builtin_amdgcn_alignbit(w0, wm1, 16), a1 = __builtin_amdgcn_alignbit(w1_, w0, 16),
                  a2 = __builtin_amdgcn_alignbit(w2_, w1_, 16), a3 = __builtin_amdgcn_alignbit(w3, w2_, 16);
        const u32 ad = sb + C_BASE + w * C_CH + 32 + (l + 64 * q) * 16;
        lds_st<u32x4>(ad + 0 * C_STR, kw[q]);
        lds_st<u32x4>(ad + 1 * C_STR, u32x4{a0, a1, a2, a3});
        lds_st<u32x4>(ad + 2 * C_STR, u32x4{wm1, w0, w1_, w2_});
        lds_st<u32x4>(ad + 3 * C_STR, u32x4{am, a0, a1, a2});
      }
#pragma unroll
      for (int q = 0; q < 4; ++q) lds_st<u32x4>(sb + U_BASE + (t & 3) * U_STR + 480 + ((t >> 2) + 64 * q) * 16, uw[q]);
      LOAD_X
      asm volatile("" ::"v"(dval));
      lds_meet(sb + GP_BASE, l);
      const int r = l & 15, g = l >> 4;
      Conv cv;
      cv.aA = sb + C_BASE + w * C_CH + (r >> 2) * C_STR + 56 + 8 * (l & 3) - 16 * g;
      cv.aB = sb + U_BASE + w * U_STR + 32 * r + 16 * g + 320;
#pragma unroll
      for (int n = 0; n < 8; ++n) {
        const u32x2 u = lds_ld<u32x2>(sb + U_BASE + w * U_STR + 480 + 512 * n + 32 * r + 8 * g);
        const u32 u0 = u[0], u1 = u[1];
        cv.acc[n] = f32x4{bflo(u0), bfhi(u0), bflo(u1), bfhi(u1)} * dval;
        if (n == 0) cv.rdall<0>();
      }
#pragma unroll
      for (int n = 0; n < 8; ++n) asm volatile("" : "+v"(cv.acc[n]));
      asm volatile("s_nop 1");
#pragma unroll 1
      for (int it = 0; it < 4; ++it) {
        __builtin_amdgcn_s_waitcnt(0xC07F);
        cv.stepk<0, 1, 0>();
        __builtin_amdgcn_s_waitcnt(0xC07F);
        cv.stepk<1, 2, 0>();
        cv.aA += 128; cv.aB -= 128;
      }
      asm volatile("s_waitcnt lgkmcnt(0)\n\ts_nop 7\n\ts_nop 1" ::: "memory");
#pragma unroll
      for (int n = 0; n < 8; ++n) asm volatile("" : "+v"(cv.acc[n]) : "v"(cv.Al[0][n]), "v"(cv.Ah[0][n]), "v"(cv.B[0][n]));
      f32x4 (&acc)[8] = cv.acc;
      asm volatile("" : "+v"(xw[0]), "+v"(xw[1]));
      const u32 ay = sb + C_BASE + w * (C_CH + 64) + 16 * r + 4 * g;
#pragma unroll
      for (int n = 0; n < 8; ++n) {
        lds_st<u32>(ay + 256 * n, pk2(acc[n][0], acc[n][2]));
        lds_st<u32>(ay + 256 * n + 2048, pk2(acc[n][1], acc[n][3]));
      }
      lds_meet(sb + GP_BASE + 4, l);
      const int q = (l & 15) >> 2, p = l & 3;
      rsrc_t ry = mkrsrc(ws + WS_YB, S_ * D_ * 2);
#pragma unroll
      for (int i = 0; i < 4; ++i) {
        const int u = w * 4 + i;
        const u32 ad = sb + C_BASE + q * (C_CH + 64) + (u * 64 + g * 16 + p * 4) * 2;
        st128<SC1>(ry, (u32)(cg * 16384 + u * 1024 + l * 16), __builtin_bit_cast(u32x4, cat8(tr_read(ad), tr_read(ad + 2048))));
      }
    } else {
      const int aw = w - 4;
      if (aw == 0) {
        __builtin_amdgcn_s_setprio(2);
        const int n0 = cg * 24 + min(l & 7, 5) * 4, which = n0 >> 10;
        const u32x4 v = ld128<0>(mkrsrc(ws + WS_MODP, 8 * 6144 * 4), (u32)(((l >> 3) * 6144 + n0) * 4));
        wg_bar();
        __builtin_amdgcn_sched_barrier(0);
        const u32 v0 = v[0], v1 = v[1], v2 = v[2], v3 = v[3];
        u32 p0 = pk2(red8(__uint_as_float(v0)), red8(__uint_as_float(v1))), p1 = pk2(red8(__uint_as_float(v2)), red8(__uint_as_float(v3)));
        if (which == 1 || which == 4) { p0 = pk2(1.f + bflo(p0), 1.f + bfhi(p0)); p1 = pk2(1.f + bflo(p1), 1.f + bfhi(p1)); }
        rsrc_t rm = mkrsrc(ws + WS_MOD, 6144 * 2);
        if (l < 6) st64<SC1>(rm, (u32)(n0 * 2), u32x2{p0, p1});
        wait_vm<0>();
        if (l < 8) flag_add(ws, FL_MOD + l * 8 + x);
        flag_wait(ws, (FL_MOD + x * 8 + (l & 7)) * 128, 32);
#pragma unroll
        for (int i = 0; i < 10; ++i) dma16<SC1>((u32)(l * 16), rm, i * 1024, sb + (i < 8 ? LN_MOD + i * 1024 : LN_MOD4 + (i - 8) * 1024));
        LOAD_X
        lds_st<volatile u32>(sb + GP_BASE + 8, 1u);
        wait_vm<0>();
        asm volatile("" : "+v"(xw[0]), "+v"(xw[1]));
        __builtin_amdgcn_s_setprio(0);
      } else {
        wg_bar();
        while ((u32)__builtin_amdgcn_readfirstlane(lds_ld<volatile u32>(sb + GP_BASE + 8)) == 0u) __builtin_amdgcn_s_sleep(4);
        LOAD_X
        asm volatile("" : "+v"(xw[0]), "+v"(xw[1]));
#pragma unroll
        for (int j = 0; j < 10; ++j) dma16<0>((u32)(l * 16), mkrsrc(w1, D_ * F_ * 2), (u32)((b * 28 + min((aw - 1) * 10 + j, 27)) * 1024), sb + GP_BASE + 1024);
      }
    }
    __builtin_amdgcn_sched_barrier(0);
    unpack8(xw[0], xv); unpack8(xw[1], xv + 8); row_stats_a(xv, sb + LN_ST, w, l);
    if (w < 4) wait_vm<0>();
    lds_bar();
    if (w == 0 && l < 8) flag_add(ws, FL_A2A + l * 8 + x);
  }
  if (w >= 4) {
    L1.init(w - 4, l, sb, ws + WS_H2F, S_ * D_ * 2, pm * 32 * 8192, w1, D_ * F_ * 2, pn * 448 - (pn & 1) * 64);
    L1.prefetchB();
    L1.rF = mkrsrc(w2, D_ * F_ * 2); L1.ldsF = sb + G1::LDS;
    L1.flag = (u32)((b * 28 + (w - 4) * 7) * 1024);
  }

  {
    float mean, rstd;
    row_stats_b(sb + LN_ST, l, mean, rstd);
    float gh[16], sv[16], cv[16];
#pragma unroll
    for (int j = 0; j < 2; ++j) {
      float sa[8], ca[8], ga[8];
      const u32 ad = sb + LN_MOD + (cj0 + j * 8) * 16;
      unpack8(lds_ld<u32x4>(ad), sa); unpack8(lds_ld<u32x4>(ad + 2048), ca); unpack8(lds_ld<u32x4>(ad + 4096), ga);
      unpack8(lds_ld<u32x4>(ad + 6144), sv + 8 * j); unpack8(lds_ld<u32x4>(ad + (LN_MOD4 - LN_MOD)), cv + 8 * j);
#pragma unroll
      for (int e = 0; e < 8; ++e) {
        xv[8 * j + e] -= mean;
        gh[8 * j + e] = ga[e] * fmaf(xv[8 * j + e] * rstd, ca[e], sa[e]);
        sv[8 * j + e] *= -1.44269504089f; cv[8 * j + e] *= -1.44269504089f;
      }
    }
#pragma unroll
    for (int e = 0; e < 16; ++e) asm volatile("" : "+v"(gh[e]), "+v"(sv[e]), "+v"(cv[e]), "+v"(xv[e]));
    if (w == 0) flag_wait(ws, (FL_A2A + x * 8 + (l & 7)) * 128, 32);
    wg_bar();
    rsrc_t ry = mkrsrc(ws + WS_YB, S_ * D_ * 2);
    u32x2 yw[2][2];
#pragma unroll
    for (int j = 0; j < 2; ++j)
#pragma unroll
      for (int h = 0; h < 2; ++h) yw[j][h] = ld64<SC1>(ry, (u32)(((2 * (cj0 + j * 8) + h) * 2048 + srow) * 8));
    const u32 fo = (u32)(pm * 32 * 8192 + (pn >> 1) * 1024 + (srow & 15) * 16);
    rsrc_t r1 = mkrsrc(ws + WS_X1F, S_ * D_ * 2), r2 = mkrsrc(ws + WS_H2F, S_ * D_ * 2);
    float s1 = 0.f, s2 = 0.f;
#pragma unroll
    for (int j = 0; j < 2; ++j) {
      float y[8];
      unpack8(u32x4{yw[j][0][0], yw[j][0][1], yw[j][1][0], yw[j][1][1]}, y);
#pragma unroll
      for (int e = 0; e < 8; ++e) { const float d = fmaf(gh[8 * j + e], y[e], xv[8 * j + e]); xv[8 * j + e] = d; s1 += d; s2 = fmaf(d, d, s2); }
    }
    s1 = red8(s1); s2 = red8(s2);
    if (l < 8) lds_st<f32x2>(sb + LN_ST + 512 + (l * 8 + w) * 8, f32x2{s1, s2});
    __builtin_amdgcn_sched_barrier(0);
#pragma unroll
    for (int j = 0; j < 2; ++j) {
      const int cj = cj0 + j * 8;
      st128<SC1>(r1, fo + (cj >> 2) * 8192 + (cj & 3) * 256, u32x4{pk2(xv[8 * j] + mean, xv[8 * j + 1] + mean), pk2(xv[8 * j + 2] + mean, xv[8 * j + 3] + mean),
                                                                   pk2(xv[8 * j + 4] + mean, xv[8 * j + 5] + mean), pk2(xv[8 * j + 6] + mean, xv[8 * j + 7] + mean)});
    }
    lds_bar();
    {
      f32x4 p = lds_ld<f32x4>(sb + LN_ST + 512 + (l & 7) * 64);
#pragma unroll
      for (int i = 1; i < 4; ++i) p += lds_ld<f32x4>(sb + LN_ST + 512 + (l & 7) * 64 + i * 16);
      const float m2 = (p[0] + p[2]) * (1.f / 1024.f);
      const float rs = __builtin_amdgcn_rsqf(fmaxf(fmaf(-m2, m2, (p[1] + p[3]) * (1.f / 1024.f)), 0.f) + 1e-5f), nm = -m2 * rs;
#pragma unroll
      for (int j = 0; j < 2; ++j) {
        const int cj = cj0 + j * 8;
        float h[8];
#pragma unroll
        for (int e = 0; e < 8; ++e) h[e] = fmaf(fmaf(xv[8 * j + e], rs, nm), cv[8 * j + e], sv[8 * j + e]);
        st128<SC1>(r2, fo + (cj >> 2) * 8192 + (cj & 3) * 256, u32x4{pk2(h[0], h[1]), pk2(h[2], h[3]), pk2(h[4], h[5]), pk2(h[6], h[7])});
      }
    }
    wait_vm<0>();
    wg_bar();
    if (w == 0 && l < 4) flag_add(ws, FL_LN + pm * 4 + l);
  }

  if (w >= 4) {
    {
      flag_wait(ws, (FL_LN + pm * 4 + (x >> 1)) * 128, 16);
      L1.run1();
      wg_bar();
    }
    {
      L2.init(w - 4, l, sb, ws + WS_HF, S_ * F_ * 2, (pm * 112 + kh * 56) * 8192, w2, F_ * D_ * 2, kh * 1792 * 2048 + (pn & 7) * 256);
      L2.ws = ws; L2.rF = mkrsrc(ws + WS_FLG, FL_LINES * 128); L2.ldsF = sb + G2::LDS + G2::SB + (w - 4) * 256;
      L2.flag = (FL_G1 + (pm * 2 + kh) * 2 + ((x >> 1) & 1)) * 128;
      L2.own = pn & 7;
      L2.prefetchB();
      wg_bar();
      L2.run2();
    }
    return;
  }
  const int wm = w >> 1, wc = w & 1, g = l >> 4;
  u32x4 pv[4];
  rsrc_t rh = mkrsrc(ws + WS_HF, S_ * F_ * 2);
  const u32 hb = (u32)(((pm * 112 + pn * 7) * 8 + wm * 4) * 1024 + l * 16);
  const int fl = FL_G1 + (pm * 2 + kh) * 2 + l;
#define STORE4(P) _Pragma("unroll") for (int m = 0; m < 4; ++m) st128<SC1>(rh, hb + (wc * 4 + (P)) * 8192 + m * 1024, pv[m]);
  const u32 ha = sb + wc * 4 * G2::SB + wm * 4096 + l * 16;
#define KEEP4(P) _Pragma("unroll") for (int m = 0; m < 4; ++m) lds_st<u32x4>(ha + (P) * G2::SB + m * 1024, pv[m]);
  {
    Consumer<G1> c;
    c.init(wm, wc, l, sb, (pn & 1) * 2);
    wg_bar();
    c.template rdall<0>();
    c.template rounds<5>();
    c.template stepk<0, 0, true, true>();
    c.template stepk<1, 0, true, false>();
    c.done();
#define SILU4(P) _Pragma("unroll") for (int m = 0; m < 4; ++m) { const f32x4 a = c.acc[m][2 * (P)], d = c.acc[m][2 * (P) + 1];          \
      pv[m] = u32x4{silu_pk(a[0], a[1]), silu_pk(a[2], a[3]), silu_pk(d[0], d[1]), silu_pk(d[2], d[3])}; asm volatile("" : "+v"(pv[m])); }
    SILU4(0) STORE4(0) KEEP4(0)
    __builtin_amdgcn_sched_barrier(0);
    u32x2 own[2];
#pragma unroll
    for (int m = 0; m < 2; ++m) {
      const f32x4 a = wc ? c.acc[m][6] : c.acc[2 + m][6], o = wc ? c.acc[2 + m][6] : c.acc[m][6];
      lds_st<u32x2>(sb + XH_BASE + (w * 2 + m) * 512 + l * 8, u32x2{silu_pk(a[0], a[1]), silu_pk(a[2], a[3])});
      own[m] = u32x2{silu_pk(o[0], o[1]), silu_pk(o[2], o[3])}; asm volatile("" : "+v"(own[m]));
    }
    SILU4(1)
    lds_bar();
    wait_vm<0>();
    if (l < 2) flag_add(ws, fl);
#pragma unroll
    for (int m = 0; m < 2; ++m) {
      const u32x2 o = lds_ld<u32x2>(sb + XH_BASE + ((w ^ 1) * 2 + m) * 512 + l * 8);
      const u32 o0 = o[0], o1 = o[1], n0 = own[m][0], n1 = own[m][1];
      const u32x4 f = wc ? u32x4{o0, o1, n0, n1} : u32x4{n0, n1, o0, o1};
      st128<SC1>(rh, hb + 3 * 8192 + (wc * 2 + m) * 1024, f);
      lds_st<u32x4>(sb + 3 * G2::SB + (wm * 4 + wc * 2 + m) * 1024 + l * 16, f);
    }
    STORE4(1) KEEP4(1)
    __builtin_amdgcn_sched_barrier(0);
    SILU4(2)
    KEEP4(2)
#undef SILU4
  }
  {
    u32x2 gbp[2], xr[4][2];
    const int jt0 = (pn & 7) * 8 + wc * 4 + kh * 2;
    Consumer<G2> c;
    c.init(wm, wc, l, sb, kh ? 0 : 2);
    lds_bar();
    c.template rdall<0>();
    c.template steps<0, 4, false>();
    wait_vm<0>();
    if (l < 2) flag_add(ws, fl + 64);
    STORE4(2)
    c.spare();
    c.template steps<4, 7>();
    c.template stepk<7, 0, false, true, 8>();
    wait_vm<0>();
    if (l < 2) flag_add(ws, fl + 128);
    {
      rsrc_t rm = mkrsrc(ws + WS_MOD, 6144 * 2), r1 = mkrsrc(ws + WS_X1F, S_ * D_ * 2);
#pragma unroll
      for (int j = 0; j < 2; ++j) {
        gbp[j] = ld64<SC1>(rm, (u32)((5 * 1024 + (jt0 + j) * 16 + 4 * g) * 2));
#pragma unroll
        for (int m = 0; m < 4; ++m)
          xr[m][j] = ld64<SC1>(r1, (u32)(((pm * 32 + (jt0 >> 1)) * 8 + wm * 4 + m) * 1024 + ((2 * j + (g >> 1)) * 16 + (l & 15)) * 16 + (g & 1) * 8));
      }
    }
    c.template rounds<6>();
    c.done();
    rsrc_t rx = mkrsrc(ws + WS_XCH, 256 * 16384);
    f32x4 keep[4][2], oth[4][2];
#pragma unroll
    for (int m = 0; m < 4; ++m) {
      f32x4 away[2];
#pragma unroll
      for (int j = 0; j < 2; ++j) { keep[m][j] = c.acc[m][2 + j]; away[j] = c.acc[m][j]; }
      st128<SC1>(rx, (u32)(b * 16384 + w * 4096 + m * 1024 + l * 16), u32x4{pk2(away[0][0], away[0][1]), pk2(away[0][2], away[0][3]), pk2(away[1][0], away[1][1]), pk2(away[1][2], away[1][3])});
    }
    wait_vm<0>();
    if (l == 0) __hip_atomic_store((GAS u32*)(ws + WS_FLG + (size_t)(FL_XC + b) * 128 + w * 4), 1u, __ATOMIC_RELAXED, __HIP_MEMORY_SCOPE_AGENT);
    float gf[2][4];
#pragma unroll
    for (int j = 0; j < 2; ++j) {
      const u32 g0 = gbp[j][0], g1 = gbp[j][1];
      gf[j][0] = bflo(g0) * -0.69314718056f; gf[j][1] = bfhi(g0) * -0.69314718056f; gf[j][2] = bflo(g1) * -0.69314718056f; gf[j][3] = bfhi(g1) * -0.69314718056f;
#pragma unroll
      for (int m = 0; m < 4; ++m) {
        const u32 x0 = xr[m][j][0], x1 = xr[m][j][1];
        keep[m][j] = f32x4{fmaf(gf[j][0], keep[m][j][0], bflo(x0)), fmaf(gf[j][1], keep[m][j][1], bfhi(x0)), fmaf(gf[j][2], keep[m][j][2], bflo(x1)), fmaf(gf[j][3], keep[m][j][3], bfhi(x1))};
        asm volatile("" : "+v"(keep[m][j]));
      }
    }
    flag_wait(ws, (FL_XC + (b ^ 4)) * 128 + w * 4, 1);
#pragma unroll
    for (int m = 0; m < 4; ++m) {
      float f[8]; unpack8(ld128<SC1>(rx, (u32)((b ^ 4) * 16384 + w * 4096 + m * 1024 + l * 16)), f);
      oth[m][0] = f32x4{f[0], f[1], f[2], f[3]}; oth[m][1] = f32x4{f[4], f[5], f[6], f[7]};
    }
#pragma unroll
    for (int m = 0; m < 4; ++m) {
      u32x2 r[2];
#pragma unroll
      for (int j = 0; j < 2; ++j) {
        const f32x4 a = keep[m][j], o = oth[m][j];
        r[j] = u32x2{pk2(fmaf(gf[j][0], o[0], a[0]), fmaf(gf[j][1], o[1], a[1])), pk2(fmaf(gf[j][2], o[2], a[2]), fmaf(gf[j][3], o[3], a[3]))};
      }
      const u32 p0 = r[0][0], p1 = r[0][1], q0 = r[1][0], q1 = r[1][1];
      auto s0 = __builtin_amdgcn_permlane16_swap(p0, q0, false, false);
      auto s1 = __builtin_amdgcn_permlane16_swap(p1, q1, false, false);
      const u32 e0 = s0[0], f0 = s0[1], e1 = s1[0], f1 = s1[1];
      st128<SC1>(mkrsrc(out, S_ * D_ * 2), (u32)(((pm * 128 + wm * 64 + m * 16 + (l & 15)) * 1024 + (jt0 + (g & 1)) * 16 + 8 * (g >> 1)) * 2), u32x4{e0, e1, f0, f1});
    }
  }
}

extern "C" void kernel_launch(void* const* d_in, const int* in_sizes, int n_in, void* d_out, int out_size, void* d_ws, size_t ws_size,
                              hipStream_t stream) {
  char* ws = (char*)d_ws;
  (void)hipFuncSetAttribute((const void*)k1_prep, hipFuncAttributeMaxDynamicSharedMemorySize, K1_LDS);
  (void)hipFuncSetAttribute((const void*)kp_main, hipFuncAttributeMaxDynamicSharedMemorySize, KP_LDS);
  k1_prep<<<256, 512, K1_LDS, stream>>>(d_in[1], d_in[2], d_in[3], d_in[4], d_in[9], d_in[10], ws);
  kp_main<<<256, 512, KP_LDS, stream>>>(d_in[0], d_in[4], d_in[5], d_in[7], d_in[8], ws, d_out);
}
```

```cpp
#define PROBE 0
#include <hip/hip_runtime.h>
#include <hip/hip_bf16.h>

typedef unsigned u32;
typedef unsigned u32x2 __attribute__((ext_vector_type(2)));
typedef unsigned u32x4 __attribute__((ext_vector_type(4)));
typedef float f32x2 __attribute__((ext_vector_type(2)));
typedef float f32x4 __attribute__((ext_vector_type(4)));
typedef __bf16 bf16x2 __attribute__((ext_vector_type(2)));
typedef __bf16 bf16x4 __attribute__((ext_vector_type(4)));
typedef __bf16 bf16x8 __attribute__((ext_vector_type(8)));
#define LAS __attribute__((address_space(3)))
#define GAS __attribute__((address_space(1)))
#define DI __device__ __forceinline__
typedef __amdgpu_buffer_rsrc_t rsrc_t;

constexpr int S_ = 2048, D_ = 1024, F_ = 3584;
#define KP_CODE_BYTES 33280
constexpr int SC1 = 16;

constexpr size_t WS_KVT = 0;
constexpr size_t WS_YB = 4u << 20;
constexpr size_t WS_X1F = 8u << 20;
constexpr size_t WS_H2F = 12u << 20;
constexpr size_t WS_HF = 16u << 20;
constexpr size_t WS_XCH = 32u << 20;
constexpr size_t WS_MOD = 36u << 20;
constexpr size_t WS_FLG = WS_MOD + (64u << 10);
constexpr size_t WS_MODP = WS_MOD + (256u << 10);
constexpr int FL_A2A = 0, FL_LN = 64, FL_MOD = 128, FL_XC = 192, FL_G1 = 192 + 256, FL_LINES = FL_G1 + 3 * 64;

#define STAMP_DECL unsigned long long st_rt = 0
#define STAMP(kid, slot) do { if ((kid) * 8 + (slot) == PROBE) { asm volatile("s_memrealtime %0\n\ts_waitcnt lgkmcnt(0)" : "=s"(st_rt) :: "memory"); } \
   if ((kid) == 0 && (slot) == 0 && blockIdx.x == 0 && threadIdx.x == 0) *(unsigned long long*)(ws + (64u << 20)) = __builtin_amdgcn_s_memrealtime(); } while (0)
#define STAMP_FLUSH
#define STAMP_END do { const unsigned long long st0 = *(volatile unsigned long long*)(ws + (64u << 20)); if (PROBE == 0) st_rt = st0; \
   for (int sp = 0; sp < 100000 && __builtin_amdgcn_s_memrealtime() < st_rt + 6000; ++sp) __builtin_amdgcn_s_sleep(1); } while (0)
#define STAMP_ARGS , unsigned long long& st_rt
#define STAMP_PASS , st_rt

extern __shared__ __attribute__((aligned(16))) char dsm[];

DI rsrc_t mkrsrc(const void* p, u32 bytes) { return __builtin_amdgcn_make_buffer_rsrc((void*)p, 0, bytes, 0x00020000); }
DI float bflo(u32 w) { return __uint_as_float(w << 16); }
DI float bfhi(u32 w) { return __uint_as_float(w & 0xffff0000u); }
DI u32 pk2(float a, float b) { f32x2 v = {a, b}; bf16x2 r = __builtin_convertvector(v, bf16x2); return __builtin_bit_cast(u32, r); }
DI float silu_f(float x) { return x * __builtin_amdgcn_rcpf(1.f + __builtin_amdgcn_exp2f(x * -1.44269504089f)); }
DI u32 silu_pk(float a, float b) {
  return pk2(a * __builtin_amdgcn_rcpf(1.f + __builtin_amdgcn_exp2f(a)), b * __builtin_amdgcn_rcpf(1.f + __builtin_amdgcn_exp2f(b)));
}
template <int AUX> DI u32x4 ld128(rsrc_t r, u32 off) { return __builtin_amdgcn_raw_buffer_load_b128(r, off, 0, AUX); }
template <int AUX> DI u32x2 ld64(rsrc_t r, u32 off) { return __builtin_amdgcn_raw_buffer_load_b64(r, off, 0, AUX); }
template <int AUX> DI void st128(rsrc_t r, u32 off, u32x4 v) { __builtin_amdgcn_raw_buffer_store_b128(v, r, off, 0, AUX); }
template <int AUX> DI void st64(rsrc_t r, u32 off, u32x2 v) { __builtin_amdgcn_raw_buffer_store_b64(v, r, off, 0, AUX); }
DI bf16x4 tr_read(u32 a) { return __builtin_amdgcn_ds_read_tr16_b64_v4bf16((LAS bf16x4*)(size_t)a); }
DI bf16x8 cat8(bf16x4 a, bf16x4 b) { return __builtin_shufflevector(a, b, 0, 1, 2, 3, 4, 5, 6, 7); }
template <class T> DI auto lds_ld(u32 a) { return *(LAS T*)(size_t)a; }
template <class T, class V> DI void lds_st(u32 a, V v) { *(LAS T*)(size_t)a = v; }
DI void unpack8(u32x4 w, float* f) {
#pragma unroll
  for (int i = 0; i < 4; ++i) { u32 x = w[i]; f[2 * i] = bflo(x); f[2 * i + 1] = bfhi(x); }
}
template <int N> DI void wait_vm() { asm volatile("s_waitcnt vmcnt(%0)" ::"n"(N) : "memory"); }
DI void wg_bar() { asm volatile("s_barrier" ::: "memory"); }
DI void lds_bar() { asm volatile("s_waitcnt lgkmcnt(0)\n\ts_barrier" ::: "memory"); }
template <int CTRL> DI float dpp_add(float x) {
  return x + __int_as_float(__builtin_amdgcn_update_dpp(0, __float_as_int(x), CTRL, 0xf, 0xf, true));
}
DI float add_x16(float x) { const u32 u = __float_as_uint(x); auto r = __builtin_amdgcn_permlane16_swap(u, u, false, false); const u32 a = r[0], b = r[1]; return __uint_as_float(a) + __uint_as_float(b); }
DI float add_x32(float x) { const u32 u = __float_as_uint(x); auto r = __builtin_amdgcn_permlane32_swap(u, u, false, false); const u32 a = r[0], b = r[1]; return __uint_as_float(a) + __uint_as_float(b); }

DI void lds_meet(u32 addr, int l) {
  asm volatile("s_waitcnt lgkmcnt(0)" ::: "memory");
  LAS u32* cnt = (LAS u32*)(size_t)addr;
  if (l == 0) __hip_atomic_fetch_add(cnt, 1u, __ATOMIC_RELAXED, __HIP_MEMORY_SCOPE_WORKGROUP);
  while ((u32)__builtin_amdgcn_readfirstlane(__hip_atomic_load(cnt, __ATOMIC_RELAXED, __HIP_MEMORY_SCOPE_WORKGROUP)) < 4u) __builtin_amdgcn_s_sleep(1);
  asm volatile("" ::: "memory");
}
DI void flag_add(char* ws, int line) { __hip_atomic_fetch_add((GAS u32*)(ws + WS_FLG + (size_t)line * 128), 1u, __ATOMIC_RELAXED, __HIP_MEMORY_SCOPE_AGENT); }
DI void flag_wait(char* ws, u32 byteoff, u32 target) {
  const GAS u32* p = (const GAS u32*)(ws + WS_FLG + byteoff);
#pragma unroll 1
  for (u32 spins = 0; spins < (1u << 18); ++spins) {
    const u32 v = __hip_atomic_load(p, __ATOMIC_RELAXED, __HIP_MEMORY_SCOPE_AGENT);
    if (__all(v >= target)) break;
    __builtin_amdgcn_s_sleep(1);
  }
  __builtin_amdgcn_fence(__ATOMIC_ACQUIRE, "wavefront");
  asm volatile("" ::: "memory");
}
DI u32 touch(rsrc_t r, u32 voff) { return __builtin_amdgcn_raw_buffer_load_b32(r, voff, 0, 0); }

template <int AUX> DI void dma16(u32 voff, rsrc_t r, u32 soff, u32 ldsaddr) {
  if constexpr (AUX == SC1) asm volatile("s_mov_b32 m0, %2\n\ts_nop 0\n\tbuffer_load_dwordx4 %0, %1, %3 offen sc1 lds" ::"v"(voff), "s"(r), "s"(ldsaddr), "s"(soff) : "memory");
  else asm volatile("s_mov_b32 m0, %2\n\ts_nop 0\n\tbuffer_load_dwordx4 %0, %1, %3 offen lds" ::"v"(voff), "s"(r), "s"(ldsaddr), "s"(soff) : "memory");
}

DI float red8(float x) { return add_x32(add_x16(dpp_add<0x128>(x))); }

constexpr int K1_GV = 64 * 320, K1_SC = K1_GV + 49152, K1_CNT = K1_SC + 256, K1_LDS = K1_CNT + 16;
__global__ __launch_bounds__(512) void k1_prep(const void* __restrict__ k, const void* __restrict__ v, const void* __restrict__ shk,
                                               const void* __restrict__ ssmk, const void* __restrict__ cin, const void* __restrict__ wmod, char* __restrict__ ws) {
  const u32 sb = (u32)(size_t)(LAS char*)dsm;
  const int t = threadIdx.x, b = blockIdx.x;
  STAMP_DECL;
  STAMP(0, 0);
  if (__builtin_amdgcn_readfirstlane(t) >= 256) {
    const int ta = t - 256, aw = __builtin_amdgcn_readfirstlane(ta >> 6), l = t & 63, idx = b >> 3;
    const int r0 = (idx >> 2) * 128, col0 = (b & 7) * 768 + (idx & 3) * 192;
    rsrc_t rw = mkrsrc(wmod, 1024 * 6144 * 2);
    const u32 cw = __builtin_amdgcn_raw_buffer_load_b16(mkrsrc(cin, 1024 * 2), (u32)((r0 + (ta & 127)) * 2), 0, 0);
    wg_bar();
#pragma unroll
    for (int i = 0; i < 3; ++i) {
      const int o = i * 1024 + l * 16, row = o / 384, cb = o % 384;
      const u32 vo = (u32)(row * 12288 + (cb ^ (((row >> 1) & 3) << 5)));
#pragma unroll
      for (int j = 0; j < 4; ++j) dma16<0>(vo, rw, (u32)((r0 + aw * 32 + j * 8) * 12288 + col0 * 2), sb + K1_GV + aw * 12288 + j * 3072 + i * 1024);
    }
    asm volatile("" ::: "memory");
    const u32 pf = touch(mkrsrc(ssmk, D_ * S_ * 2), (u32)(((b & 7) * 32 + idx) * 16384 + (ta & 127) * 128));
    u32 lo, hi;
    asm volatile("s_getpc_b64 s[20:21]\n\ts_add_u32 s20, s20, kp_main@rel32@lo+4\n\ts_addc_u32 s21, s21, kp_main@rel32@hi+12\n\ts_mov_b32 %0, s20\n\ts_mov_b32 %1, s21"
                 : "=s"(lo), "=s"(hi) : : "s20", "s21", "scc");
    const u32 pc = touch(mkrsrc((const void*)(((unsigned long long)hi << 32) | lo), KP_CODE_BYTES), (u32)((idx * 16 + (t & 15)) * 128));
    wait_vm<2>();
    lds_st<unsigned short>(sb + K1_SC + (ta & 127) * 2, (unsigned short)pk2(silu_f(bflo(cw)), 0.f));
    STAMP(0, 1);
    lds_meet(sb + K1_CNT + 4, l);
    const int g = l >> 4, row = 4 * g + ((l & 15) >> 2);
    f32x4 d[3] = {{0, 0, 0, 0}, {0, 0, 0, 0}, {0, 0, 0, 0}};
#pragma unroll
    for (int ks = 0; ks < 4; ++ks) {
      const bf16x8 sc = cat8(lds_ld<bf16x4>(sb + K1_SC + ks * 64 + g * 8), lds_ld<bf16x4>(sb + K1_SC + ks * 64 + 32 + g * 8));
#pragma unroll
      for (int j = 0; j < 3; ++j) {
        const u32 ad = sb + K1_GV + ks * 12288 + row * 384 + (((aw * 3 + j) ^ ((row >> 1) & 3)) << 5) + (l & 3) * 8;
        d[j] = __builtin_amdgcn_mfma_f32_16x16x32_bf16(sc, cat8(tr_read(ad), tr_read(ad + 16 * 384)), d[j], 0, 0, 0);
      }
    }
    if (g < 3) ((float*)(ws + WS_MODP))[(idx >> 2) * 6144 + col0 + aw * 48 + l] = g == 0 ? d[0][0] : g == 1 ? d[1][0] : d[2][0];
    asm volatile("" ::"v"(pf), "v"(pc));
    STAMP(0, 7);
    STAMP_FLUSH;
    return;
  }
  const int dblk = b & 7, sblk = b >> 3;
  const int a = t >> 4, cc = t & 15;
  const int s0 = sblk * 64 + a * 4, ch0 = dblk * 128 + cc * 8;
  rsrc_t rk = mkrsrc(k, S_ * D_ * 2), rv = mkrsrc(v, S_ * D_ * 2), rs = mkrsrc(shk, D_ * 4 * 2);
  u32x4 kk[7], vv[4], sh[4];
#pragma unroll
  for (int j = 0; j < 7; ++j) kk[j] = ld128<0>(rk, (u32)((s0 - 3 + j) * 2048 + ch0 * 2));
#pragma unroll
  for (int j = 0; j < 4; ++j) vv[j] = ld128<0>(rv, (u32)((s0 + j) * 2048 + ch0 * 2));
#pragma unroll
  for (int j = 0; j < 4; ++j) sh[j] = ld128<0>(rs, (u32)(ch0 * 8 + j * 16));
  if (t == 0) lds_st<u32x2>(sb + K1_CNT, u32x2{0u, 0u});
  __builtin_amdgcn_sched_barrier(0);
  lds_bar();
  __builtin_amdgcn_sched_barrier(0);
  float shf[8][4];
#pragma unroll
  for (int j = 0; j < 4; ++j) { float f[8]; unpack8(sh[j], f);
#pragma unroll
    for (int i = 0; i < 8; ++i) shf[2 * j + (i >> 2)][i & 3] = f[i]; }
  float kf[7][8];
#pragma unroll
  for (int j = 0; j < 7; ++j) unpack8(kk[j], kf[j]);
#pragma unroll
  for (int r = 0; r < 4; ++r) {
    float vf[8], o[8]; unpack8(vv[r], vf);
#pragma unroll
    for (int e = 0; e < 8; ++e) {
      float x = kf[r][e] * shf[e][3];
      x = fmaf(kf[r + 1][e], shf[e][2], x);
      x = fmaf(kf[r + 2][e], shf[e][1], x);
      x = fmaf(kf[r + 3][e], shf[e][0], x);
      o[e] = x * vf[e];
    }
    u32x4 w = {pk2(o[0], o[1]), pk2(o[2], o[3]), pk2(o[4], o[5]), pk2(o[6], o[7])};
    lds_st<u32x4>(sb + (a * 4 + r) * 320 + cc * 16, w);
  }
  STAMP(0, 1);
  const int w = t >> 6, l = t & 63, g = l >> 4, q = (l & 15) >> 2, p = l & 3;
  lds_meet(sb + K1_CNT, l);
  rsrc_t rd = mkrsrc(ws + WS_KVT, S_ * D_ * 2);
#pragma unroll
  for (int i = 0; i < 4; ++i) {
    const int u = w * 4 + i, s8 = u >> 1, hf = u & 1;
    const u32 ad = sb + (s8 * 8 + q) * 320 + (hf * 64 + g * 16 + p * 4) * 2;
    bf16x8 r = cat8(tr_read(ad), tr_read(ad + 4 * 320));
    st128<0>(rd, (u32)((((sblk * 4 + (s8 >> 1)) * 256 + dblk * 32 + hf * 16 + (l >> 2)) * 8 + (s8 & 1) * 4 + (l & 3)) * 16), __builtin_bit_cast(u32x4, r));
  }
  if (t < 3 && b * 3 + t < FL_LINES) *(u32x4*)(ws + WS_FLG + (size_t)(b * 3 + t) * 128) = u32x4{0, 0, 0, 0};
  STAMP(0, 7);
  STAMP_FLUSH;
}

template <int MODE_, int NT_, int KS_, int NS_, int LDB_, int PB_>
struct GC {
  static constexpr int MODE = MODE_, NT = NT_, KS = KS_, NS = NS_, SY = 2, LDB = LDB_;
  static constexpr int PB = PB_;
  static constexpr int OWN = MODE == 1 ? 0 : 7;
  static constexpr int NPB = MODE == 1 ? 4 : 2;
  static constexpr int PSTR = MODE == 1 ? 1088 : 1024, SB = 8192 + 4 * NPB * PSTR, NB = KS / SY, LDS = NS * SB;
  static constexpr int mn(int a, int b) { return a < b ? a : b; }
  static constexpr int need(int j) { return mn(SY * (j + 1), KS - 1); }
  static constexpr int issued(int j) { return j <= 0 ? NS - 1 : mn(SY * (j - 1) + NS, KS - 1); }
  static constexpr int cnt(int a, int b) { int n = 0; for (int s = a; s <= b; ++s) n += s < PB ? 2 : 2 + NPB; return n; }
  static constexpr int prod(int s) { return s < 21 ? (s - 7) >> 1 : s < 42 ? (s - 21) / 3 : (s - 42) >> 1; }
  static constexpr int kloc(int s) { return MODE == 1 || s < 7 ? s : s < 21 ? 4 * ((s - 7) & 1) : s < 42 ? 1 + 2 * ((s - 21) % 3) : 2 + 4 * (s & 1); }
  static constexpr int gate(int j) { return MODE == 1 || j < 0 ? 0 : (issued(j) < 21 && issued(j + 1) >= 21) ? 1 : (issued(j) < 42 && issued(j + 1) >= 42) ? 2 : 0; }
  static constexpr int slot(int s) { return MODE == 2 && s == NS ? NS : s % NS; }
  static constexpr int PD = (NS + SY) / SY - 2;
};

template <class C> struct Loader {
  u32 voffA, voffB; rsrc_t rA, rB, rF; u32 soffA, soffB, ldsA, ldsB, ldsF, flag; int own; char* ws;
  template <int ST> DI u32 kstep() {
    if constexpr (C::MODE == 1) return ST;
    else if constexpr (ST < C::OWN) return own * 7 + ST;
    else return (C::prod(ST) >= own ? 7 : 0) + C::prod(ST) * 7 + C::kloc(ST);
  }
  template <int ST> DI void stA() {
    const u32 so = soffA + kstep<ST>() * 8192;
#pragma unroll
    for (int i = 0; i < 2; ++i) dma16<SC1>(voffA, rA, so + i * 1024, ldsA + C::slot(ST) * C::SB + i * 1024);
  }
  template <int ST> DI void stB() {
    const u32 so = soffB + kstep<ST>() * (32 * C::LDB);
#pragma unroll
    for (int i = 0; i < C::NPB; ++i) {
      if constexpr (C::MODE == 1) dma16<0>(voffB, rB, so + 2 * i * C::LDB, ldsB + C::slot(ST) * C::SB + ((i & 1) + 4 * (i >> 1)) * C::PSTR);
      else dma16<0>(voffB ^ (i * 512), rB, so + 4 * i * C::LDB, ldsB + C::slot(ST) * C::SB + i * 1024);
    }
  }
  template <int A, int B, int WHAT> DI void stages() { if constexpr (A <= B) { if constexpr (WHAT & 1) stA<A>(); if constexpr (WHAT & 2) stB<A>(); stages<A + 1, B, WHAT>(); } }
  template <int J> DI void step(int dummy STAMP_ARGS) {
    if constexpr (J < C::NB) {
      wait_vm<C::cnt(C::need(J) + 1, C::issued(J)) + (C::MODE == 1 && C::issued(J) == C::KS - 1 ? 7 : 0)>();
      wg_bar();
      if constexpr (C::gate(J + C::PD) != 0) {
        static_assert(C::issued(J) + 1 <= C::need(J + C::PD), "the poll must be older than the stage the later wait covers");
        asm volatile("s_mov_b32 m0, %2\n\ts_nop 0\n\tbuffer_load_dword %0, %1, %3 offen sc1 lds" ::"v"(0u), "s"(rF), "s"(ldsF), "s"(flag + C::gate(J + C::PD) * 64 * 128) : "memory");
      }
      if constexpr (C::gate(J) != 0) {
        STAMP(4, 2 * C::gate(J) - 1 + (C::gate(J) - 1));
        if ((u32)__builtin_amdgcn_readfirstlane(lds_ld<volatile u32>(ldsF)) < 32u) flag_wait(ws, flag + C::gate(J) * 64 * 128, 32);
        STAMP(4, 2 * C::gate(J) + (C::gate(J) - 1));
      }
      stages<C::issued(J) + 1, C::issued(J + 1), 3>();
      if constexpr (C::MODE == 1 && C::issued(J) < C::KS - 1 && C::issued(J + 1) == C::KS - 1) {
#pragma unroll
        for (int i = 0; i < 7; ++i) dma16<0>(voffA, rF, flag + i * 1024, ldsF);
      }
      step<J + 1>(0 STAMP_PASS);
    }
  }
  DI void init(int lw, int l, u32 sb, const void* Aptr, u32 Abytes, u32 aoff, const void* Bptr, u32 Bbytes, u32 boff) {
    rA = mkrsrc(Aptr, Abytes); rB = mkrsrc(Bptr, Bbytes);
    voffA = l * 16; soffA = aoff + lw * 2048; ldsA = sb + lw * 2048;
    soffB = boff + lw * 8 * C::LDB;
    if constexpr (C::MODE == 1) {
      const int h = l >> 5;
      voffB = h * C::LDB + (((l & 31) * 16) ^ (h * 32));
      ldsB = sb + 8192 + (2 * (lw & 1) + 8 * (lw >> 1)) * C::PSTR;
    } else {
      const int h = l >> 4;
      const int sg = ((l & 15) >> 1) ^ h;
      voffB = h * C::LDB + (sg >> 2) * 512 + (sg & 3) * 32 + (l & 1) * 16;
      ldsB = sb + 8192 + lw * 2048;
    }
  }
  DI void prefetchB() { stages<0, C::PB - 1, 2>(); }
  DI void run1(int dummy STAMP_ARGS) {
    stages<0, 1, 1>();
    wait_vm<2>();
    wg_bar();
    stages<2, C::PB - 1, 1>();
    stages<C::PB, C::NS - 1, 3>();
    step<0>(0 STAMP_PASS);
  }
  DI void run2(int dummy STAMP_ARGS) {
    static_assert(C::PB == C::NS + 1 && C::OWN == C::NS - 1 && C::gate(2 + C::PD) == 0, "start-up");
    wait_vm<0>();
    wg_bar();
    flag_wait(ws, flag, 32);
    STAMP(4, 3);
    stages<C::OWN, C::NS, 1>();
    wg_bar();
    stages<C::NS + 1, C::issued(3), 3>();
    step<3>(0 STAMP_PASS);
  }
};

template <class C> struct Consumer {
  static constexpr int NT = C::NT, NBASE = C::MODE == 1 ? 3 : NT, NR = 4 + 2 * NT, NM = 4 * NT, PRE = C::MODE == 1 ? 2 : 4;
  static constexpr int TOFF = C::MODE == 1 ? 4 * C::PSTR : 4096, WIN = C::NS / 2;
  static_assert(PRE + NR <= NM && C::NS % 2 == 0 && (WIN - 1) * C::SB + TOFF + 4096 < 65536, "slots");
  f32x4 acc[4][NT];
  u32x4 Af[2][4]; u32x2 Bl[2][NT], Bh[2][NT];
  u32 ac[3], bc[3][NBASE];
  template <int X, int SL, int R> DI void rd1() {
    constexpr int wi = SL / WIN, so = (SL % WIN) * C::SB;
    if constexpr (R >= 2 && R < 6) asm volatile("ds_read_b128 %0, %1 offset:%2" : "=v"(Af[X][R - 2]) : "v"(ac[wi]), "i"(so + (R - 2) * 1024) : "memory");
    else if constexpr (R < NR) {
      constexpr int q = R < 2 ? R : R - 4, j = q >> 1, h = q & 1, bi = C::MODE == 1 ? (j == 6 ? 2 : (j & 1)) : j, off = so + (C::MODE == 1 && j < 6 ? 32 * j : 0) + h * TOFF;
      if constexpr (h == 0) asm volatile("ds_read_b64_tr_b16 %0, %1 offset:%2" : "=v"(Bl[X][j]) : "v"(bc[wi][bi]), "i"(off) : "memory");
      else asm volatile("ds_read_b64_tr_b16 %0, %1 offset:%2" : "=v"(Bh[X][j]) : "v"(bc[wi][bi]), "i"(off) : "memory");
    }
  }
  template <int X, int K> DI void mm1() {
    constexpr int j = K >> 2, m = (j & 1) ? 3 - (K & 3) : (K & 3);
    const u32x4 b = {Bl[X][j][0], Bl[X][j][1], Bh[X][j][0], Bh[X][j][1]};
    asm volatile("v_mfma_f32_16x16x32_bf16 %0, %1, %2, %0" : "+v"(acc[m][j]) : "v"(b), "v"(Af[X][m]));
  }
  static constexpr int lastrd(int K) { int r = -1; for (int k = 0; k <= K; ++k) { const int j = k >> 2, m = (j & 1) ? 3 - (k & 3) : (k & 3), rb = j == 0 ? 1 : 2 * j + 5, ra = 2 + m; r = rb > r ? rb : r; r = ra > r ? ra : r; } return r; }
  template <int N> DI void wl() { __builtin_amdgcn_s_waitcnt(0xC07F | ((N > 15 ? 15 : N) << 8)); }
  template <int ST, int K, bool BAR, bool RD, int SL = (ST + 1) % C::NS> DI void stepk() {
    if constexpr (K < NM) {
      if constexpr (K == 0 && PRE > 0) wl<NR - 1 - lastrd(PRE - 1)>();
      if constexpr (K == PRE) { wl<0>(); if constexpr (BAR) wg_bar(); }
      mm1<ST & 1, K>();
      if constexpr (RD && K >= PRE) rd1<(ST & 1) ^ 1, SL, K - PRE>();
      stepk<ST, K + 1, BAR, RD, SL>();
    }
  }
  template <int A, int B, bool BAR = true> DI void steps() { if constexpr (A < B) { stepk<A, 0, BAR && !(A & 1), true>(); steps<A + 1, B, BAR>(); } }
  template <int R> DI void rdall() { if constexpr (R < NR) { rd1<0, 0, R>(); rdall<R + 1>(); } }
  DI void init(int wm, int wc, int l, u32 sb, int coff) {
    const int g = l >> 4, q = (l & 15) >> 2, p = l & 3;
    ac[0] = sb + wm * 4096 + l * 16;
    if constexpr (C::MODE == 1) {
      const int base = sb + 8192 + ((q >> 1) + 2 * (g & 1) + 8 * (g >> 1)) * C::PSTR + (q & 1) * 512 + 8 * p + 32 * (8 * wc + coff);
      const int dl = (q & 1) * 32;
      bc[0][0] = base + dl;
      bc[0][1] = base - dl;
      bc[0][2] = wc ? base - 32 - dl : base + 192 + dl;
    } else {
      const int row = 4 * g + q;
#pragma unroll
      for (int j = 0; j < NT; ++j) bc[0][j] = sb + 8192 + row * 256 + 8 * p + 32 * ((NT * wc + (j ^ coff)) ^ (row & 7));
    }
    ac[1] = ac[0] + WIN * C::SB;
    asm volatile("" : "+v"(ac[0]), "+v"(ac[1]));
#pragma unroll
    for (int i = 0; i < NBASE; ++i) { bc[1][i] = bc[0][i] + WIN * C::SB; asm volatile("" : "+v"(bc[0][i]), "+v"(bc[1][i])); }
#pragma unroll
    for (int m = 0; m < 4; ++m)
#pragma unroll
      for (int j = 0; j < NT; ++j) { acc[m][j] = f32x4{0, 0, 0, 0}; asm volatile("" : "+v"(acc[m][j])); }
  }
  DI void spare() {
    ac[2] = ac[0] + C::LDS; asm volatile("" : "+v"(ac[2]));
#pragma unroll
    for (int i = 0; i < NBASE; ++i) { bc[2][i] = bc[0][i] + C::LDS; asm volatile("" : "+v"(bc[2][i])); }
  }
  template <int N> DI void rounds() {
#pragma unroll 1
    for (int j = 0; j < N; ++j) steps<0, C::NS>();
  }
  DI void done() {
    asm volatile("s_waitcnt lgkmcnt(0)\n\ts_nop 7\n\ts_nop 1" ::: "memory");
#pragma unroll
    for (int m = 0; m < 4; ++m)
#pragma unroll
      for (int j = 0; j < NT; ++j) asm volatile("" : "+v"(acc[m][j]));
#pragma unroll
    for (int m = 0; m < 4; ++m) asm volatile("" ::"v"(Af[0][m]), "v"(Bl[0][m]), "v"(Bh[0][m]));
  }
};
typedef GC<1, 7, 32, 6, F_ * 2, 2> G1;
typedef GC<2, 4, 56, 8, D_ * 2, 9> G2;

constexpr int U_BASE = 128, U_STR = 4576, C_STR = 4160, C_CH = 4 * C_STR, C_BASE = U_BASE + 4 * U_STR, GP_BASE = C_BASE + 4 * C_CH + 256;
constexpr int LN_MOD = 4 * G1::SB, LN_MOD4 = 5 * G1::SB, LN_ST = LN_MOD4 + 2048;
static_assert(LN_MOD >= GP_BASE + 256 && G1::NS >= 6, "adaLN scratch");
constexpr int XH_BASE = G2::LDS + G2::SB + 1024;
constexpr int KP_LDS = G1::LDS + 1024;
static_assert(KP_LDS >= XH_BASE + 4096, "LDS");
static_assert(KP_LDS <= 163840, "LDS");

struct Conv {
  f32x4 acc[8]; u32x2 Al[2][8], Ah[2][8]; u32x4 B[2][8]; u32 aA, aB;
  static constexpr int seq(int k, bool tile) {
    int c = 0;
    for (int r = 0; r < 8; ++r) {
      const int n[5] = {7, r < 7 ? 6 : 0, r < 6 ? 5 : 1, r < 5 ? 4 : 2, 3}, i[5] = {r, r < 7 ? r : 0, r < 6 ? r : r - 6, r < 5 ? r : r - 5, r >> 1};
      for (int e = 0; e < ((r & 1) ? 4 : 5); ++e) { if (c == k) return tile ? n[e] : i[e]; ++c; }
    }
    return 0;
  }
  template <int X, int T0, int R> DI void rd1() {
    constexpr int i = R / 3, k = R % 3;
    if constexpr (k == 0) asm volatile("ds_read_b64_tr_b16 %0, %1 offset:%2" : "=v"(Al[X][i]) : "v"(aA), "i"(64 * (8 * i + T0) + 8) : "memory");
    if constexpr (k == 1) asm volatile("ds_read_b64_tr_b16 %0, %1 offset:%2" : "=v"(Ah[X][i]) : "v"(aA), "i"(64 * (8 * i + T0)) : "memory");
    if constexpr (k == 2) asm volatile("ds_read_b128 %0, %1 offset:%2" : "=v"(B[X][7 - i]) : "v"(aB), "i"(512 * (7 - i) + 64 * (2 - T0)) : "memory");
  }
  template <int X, int T0, int K> DI void stepk() {
    if constexpr (K < 36) {
      constexpr int n = seq(K, true), i = seq(K, false);
      const u32x4 a = {Al[X][i][0], Al[X][i][1], Ah[X][i][0], Ah[X][i][1]};
      asm volatile("v_mfma_f32_16x16x32_bf16 %0, %1, %2, %0" : "+v"(acc[n]) : "v"(a), "v"(B[X][n - i]));
      if constexpr (K < 24) rd1<X ^ 1, T0, K>();
      stepk<X, T0, K + 1>();
    }
  }
  template <int R> DI void rdall() { if constexpr (R < 24) { rd1<0, 0, R>(); rdall<R + 1>(); } }
};

DI void row_stats_a(const float (&x)[16], u32 slot, int w, int l) {
  float s = 0.f;
#pragma unroll
  for (int e = 0; e < 16; ++e) s += x[e];
  const float mw = red8(s) * (1.f / 128.f);
  float qv = 0.f;
#pragma unroll
  for (int e = 0; e < 16; ++e) { const float d = x[e] - mw; qv = fmaf(d, d, qv); }
  qv = red8(qv);
  if (l < 8) lds_st<f32x2>(slot + (l * 8 + w) * 8, f32x2{mw, qv});
}
DI void row_stats_b(u32 slot, int l, float& mean, float& rstd) {
  f32x4 p[4];
#pragma unroll
  for (int i = 0; i < 4; ++i) p[i] = lds_ld<f32x4>(slot + (l & 7) * 64 + i * 16);
  float m = 0.f;
#pragma unroll
  for (int i = 0; i < 4; ++i) m += p[i][0] + p[i][2];
  m *= 0.125f;
  float m2 = 0.f, dd = 0.f;
#pragma unroll
  for (int i = 0; i < 4; ++i) { m2 += p[i][1] + p[i][3]; const float d0 = p[i][0] - m, d1 = p[i][2] - m; dd = fmaf(d0, d0, dd); dd = fmaf(d1, d1, dd); }
  mean = m;
  rstd = __builtin_amdgcn_rsqf(fmaf(dd, 128.f, m2) * (1.f / 1024.f) + 1e-5f);
}

extern "C" __global__ __launch_bounds__(512, 2) void kp_main(const void* __restrict__ xin, const void* __restrict__ ssmk, const void* __restrict__ ssmd,
                                                             const void* __restrict__ w1, const void* __restrict__ w2, char* __restrict__ ws, void* __restrict__ out) {
  const u32 sb = (u32)(size_t)(LAS char*)dsm;
  const int t = threadIdx.x, b = blockIdx.x, w = __builtin_amdgcn_readfirstlane(t >> 6), l = t & 63;
  const int x = b & 7, idx = b >> 3, cg = x * 32 + idx;
  const int pm = (x & 1) * 8 + (idx & 7), pn = (x >> 1) * 4 + (idx >> 3), kh = x >> 2;
  const int pq = (pn & 7) ^ (pm & 3), pc = (pq & 3) + (pq >> 2) * 8;
  const int srow = pm * 128 + pn * 8 + (l & 7), cj0 = w * 16 + (l >> 3);
  STAMP_DECL;
  u32x4 xw[2];
#define LOAD_X _Pragma("unroll") for (int j = 0; j < 2; ++j) xw[j] = ld128<0>(mkrsrc(xin, S_ * D_ * 2), (u32)(srow * 2048 + (cj0 + j * 8) * 16));
  float xv[16];
  Loader<G1> L1; Loader<G2> L2;

  {
    STAMP(1, 0);
    if (w < 4) {
      const int ch = cg * 4 + w;
      rsrc_t rk = mkrsrc(ssmk, D_ * S_ * 2);
      u32x4 kw[4]; u32x2 khl[4];
#pragma unroll
      for (int q = 0; q < 4; ++q) {
        const u32 o = (u32)(ch * 4096 + (l + 64 * q) * 16);
        kw[q] = ld128<0>(rk, o);
        khl[q] = ld64<0>(rk, o - 8);
      }
      u32x4 uw[4];
#pragma unroll
      for (int q = 0; q < 4; ++q) uw[q] = ld128<0>(mkrsrc(ws + WS_KVT, S_ * D_ * 2), (u32)((((t >> 3) + 32 * q) * 256 + cg) * 128 + (t & 7) * 16));
      const float dval = bflo(((const unsigned short*)ssmd)[ch]);
      if (t == 0) lds_st<u32x4>(sb + GP_BASE, u32x4{0u, 0u, 0u, 0u});
      __builtin_amdgcn_sched_barrier(0);
      lds_bar();
      __builtin_amdgcn_sched_barrier(0);
      const u32x4 z4 = {0, 0, 0, 0};
      if (l < 30) lds_st<u32x4>(sb + U_BASE + w * U_STR + l * 16, z4);
      if (l < 8) lds_st<u32x4>(sb + C_BASE + w * C_CH + (l >> 1) * C_STR + (l & 1) * 16, z4);
      if (l == 0) khl[0] = u32x2{0, 0};
#pragma unroll
      for (int q = 0; q < 4; ++q) {
        const u32 wm2 = khl[q][0], wm1 = khl[q][1], w0 = kw[q][0], w1_ = kw[q][1], w2_ = kw[q][2], w3 = kw[q][3];
        const u32 am = __builtin_amdgcn_alignbit(wm1, wm2, 16), a0 = __builtin_amdgcn_alignbit(w0, wm1, 16), a1 = __builtin_amdgcn_alignbit(w1_, w0, 16),
                  a2 = __builtin_amdgcn_alignbit(w2_, w1_, 16), a3 = __builtin_amdgcn_alignbit(w3, w2_, 16);
        const u32 ad = sb + C_BASE + w * C_CH + 32 + (l + 64 * q) * 16;
        lds_st<u32x4>(ad + 0 * C_STR, kw[q]);
        lds_st<u32x4>(ad + 1 * C_STR, u32x4{a0, a1, a2, a3});
        lds_st<u32x4>(ad + 2 * C_STR, u32x4{wm1, w0, w1_, w2_});
        lds_st<u32x4>(ad + 3 * C_STR, u32x4{am, a0, a1, a2});
      }
#pragma unroll
      for (int q = 0; q < 4; ++q) lds_st<u32x4>(sb + U_BASE + (t & 3) * U_STR + 480 + ((t >> 2) + 64 * q) * 16, uw[q]);
      STAMP(1, 1);
      LOAD_X
      asm volatile("" ::"v"(dval));
      lds_meet(sb + GP_BASE, l);
      STAMP(1, 2);
      const int r = l & 15, g = l >> 4;
      Conv cv;
      cv.aA = sb + C_BASE + w * C_CH + (r >> 2) * C_STR + 56 + 8 * (l & 3) - 16 * g;
      cv.aB = sb + U_BASE + w * U_STR + 32 * r + 16 * g + 320;
#pragma unroll
      for (int n = 0; n < 8; ++n) {
        const u32x2 u = lds_ld<u32x2>(sb + U_BASE + w * U_STR + 480 + 512 * n + 32 * r + 8 * g);
        const u32 u0 = u[0], u1 = u[1];
        cv.acc[n] = f32x4{bflo(u0), bfhi(u0), bflo(u1), bfhi(u1)} * dval;
        if (n == 0) cv.rdall<0>();
      }
#pragma unroll
      for (int n = 0; n < 8; ++n) asm volatile("" : "+v"(cv.acc[n]));
      asm volatile("s_nop 1");
#pragma unroll 1
      for (int it = 0; it < 4; ++it) {
        __builtin_amdgcn_s_waitcnt(0xC07F);
        cv.stepk<0, 1, 0>();
        __builtin_amdgcn_s_waitcnt(0xC07F);
        cv.stepk<1, 2, 0>();
        cv.aA += 128; cv.aB -= 128;
      }
      asm volatile("s_waitcnt lgkmcnt(0)\n\ts_nop 7\n\ts_nop 1" ::: "memory");
#pragma unroll
      for (int n = 0; n < 8; ++n) asm volatile("" : "+v"(cv.acc[n]) : "v"(cv.Al[0][n]), "v"(cv.Ah[0][n]), "v"(cv.B[0][n]));
      f32x4 (&acc)[8] = cv.acc;
      STAMP(1, 3);
      asm volatile("" : "+v"(xw[0]), "+v"(xw[1]));
      const u32 ay = sb + C_BASE + w * (C_CH + 64) + 16 * r + 4 * g;
#pragma unroll
      for (int n = 0; n < 8; ++n) {
        lds_st<u32>(ay + 256 * n, pk2(acc[n][0], acc[n][2]));
        lds_st<u32>(ay + 256 * n + 2048, pk2(acc[n][1], acc[n][3]));
      }
      lds_meet(sb + GP_BASE + 4, l);
      STAMP(1, 4);
      const int q = (l & 15) >> 2, p = l & 3;
      rsrc_t ry = mkrsrc(ws + WS_YB, S_ * D_ * 2);
#pragma unroll
      for (int i = 0; i < 4; ++i) {
        const int u = w * 4 + i;
        const u32 ad = sb + C_BASE + q * (C_CH + 64) + (u * 64 + g * 16 + p * 4) * 2;
        st128<SC1>(ry, (u32)(cg * 16384 + u * 1024 + l * 16), __builtin_bit_cast(u32x4, cat8(tr_read(ad), tr_read(ad + 2048))));
      }
    } else {
      const int aw = w - 4;
      if (aw == 0) {
        __builtin_amdgcn_s_setprio(2);
        const int n0 = cg * 24 + min(l & 7, 5) * 4, which = n0 >> 10;
        const u32x4 v = ld128<0>(mkrsrc(ws + WS_MODP, 8 * 6144 * 4), (u32)(((l >> 3) * 6144 + n0) * 4));
        wg_bar();
        __builtin_amdgcn_sched_barrier(0);
        const u32 v0 = v[0], v1 = v[1], v2 = v[2], v3 = v[3];
        u32 p0 = pk2(red8(__uint_as_float(v0)), red8(__uint_as_float(v1))), p1 = pk2(red8(__uint_as_float(v2)), red8(__uint_as_float(v3)));
        if (which == 1 || which == 4) { p0 = pk2(1.f + bflo(p0), 1.f + bfhi(p0)); p1 = pk2(1.f + bflo(p1), 1.f + bfhi(p1)); }
        rsrc_t rm = mkrsrc(ws + WS_MOD, 6144 * 2);
        if (l < 6) st64<SC1>(rm, (u32)(n0 * 2), u32x2{p0, p1});
        wait_vm<0>();
        if (l < 8) flag_add(ws, FL_MOD + l * 8 + x);
        STAMP(1, 1);
        flag_wait(ws, (FL_MOD + x * 8 + (l & 7)) * 128, 32);
        STAMP(1, 2);
#pragma unroll
        for (int i = 0; i < 10; ++i) dma16<SC1>((u32)(l * 16), rm, i * 1024, sb + (i < 8 ? LN_MOD + i * 1024 : LN_MOD4 + (i - 8) * 1024));
        LOAD_X
        lds_st<volatile u32>(sb + GP_BASE + 8, 1u);
        wait_vm<0>();
        asm volatile("" : "+v"(xw[0]), "+v"(xw[1]));
        __builtin_amdgcn_s_setprio(0);
      } else {
        wg_bar();
        while ((u32)__builtin_amdgcn_readfirstlane(lds_ld<volatile u32>(sb + GP_BASE + 8)) == 0u) __builtin_amdgcn_s_sleep(4);
        LOAD_X
        asm volatile("" : "+v"(xw[0]), "+v"(xw[1]));
#pragma unroll
        for (int j = 0; j < 10; ++j) dma16<0>((u32)(l * 16), mkrsrc(w1, D_ * F_ * 2), (u32)((b * 28 + min((aw - 1) * 10 + j, 27)) * 1024), sb + GP_BASE + 1024);
      }
      STAMP(1, 3);
    }
    __builtin_amdgcn_sched_barrier(0);
    unpack8(xw[0], xv); unpack8(xw[1], xv + 8); row_stats_a(xv, sb + LN_ST, w, l);
    if (w < 4) wait_vm<0>();
    lds_bar();
    if (w == 0 && l < 8) flag_add(ws, FL_A2A + l * 8 + x);
    STAMP(1, 7);
  }
  if (w >= 4) {
    L1.init(w - 4, l, sb, ws + WS_H2F, S_ * D_ * 2, pm * 32 * 8192, w1, D_ * F_ * 2, pn * 448 - (pn & 1) * 64);
    L1.prefetchB();
    L1.rF = mkrsrc(w2, D_ * F_ * 2); L1.ldsF = sb + G1::LDS;
    L1.flag = (u32)((b * 28 + (w - 4) * 7) * 1024);
  }

  {
    STAMP(2, 0);
    float mean, rstd;
    row_stats_b(sb + LN_ST, l, mean, rstd);
    float gh[16], sv[16], cv[16];
#pragma unroll
    for (int j = 0; j < 2; ++j) {
      float sa[8], ca[8], ga[8];
      const u32 ad = sb + LN_MOD + (cj0 + j * 8) * 16;
      unpack8(lds_ld<u32x4>(ad), sa); unpack8(lds_ld<u32x4>(ad + 2048), ca); unpack8(lds_ld<u32x4>(ad + 4096), ga);
      unpack8(lds_ld<u32x4>(ad + 6144), sv + 8 * j); unpack8(lds_ld<u32x4>(ad + (LN_MOD4 - LN_MOD)), cv + 8 * j);
#pragma unroll
      for (int e = 0; e < 8; ++e) {
        xv[8 * j + e] -= mean;
        gh[8 * j + e] = ga[e] * fmaf(xv[8 * j + e] * rstd, ca[e], sa[e]);
        sv[8 * j + e] *= -1.44269504089f; cv[8 * j + e] *= -1.44269504089f;
      }
    }
#pragma unroll
    for (int e = 0; e < 16; ++e) asm volatile("" : "+v"(gh[e]), "+v"(sv[e]), "+v"(cv[e]), "+v"(xv[e]));
    STAMP(2, 1);
    if (w == 0) flag_wait(ws, (FL_A2A + x * 8 + (l & 7)) * 128, 32);
    wg_bar();
    STAMP(2, 2);
    rsrc_t ry = mkrsrc(ws + WS_YB, S_ * D_ * 2);
    u32x2 yw[2][2];
#pragma unroll
    for (int j = 0; j < 2; ++j)
#pragma unroll
      for (int h = 0; h < 2; ++h) yw[j][h] = ld64<SC1>(ry, (u32)(((2 * (cj0 + j * 8) + h) * 2048 + srow) * 8));
    const u32 fo = (u32)(pm * 32 * 8192 + (pn >> 1) * 1024 + (srow & 15) * 16);
    rsrc_t r1 = mkrsrc(ws + WS_X1F, S_ * D_ * 2), r2 = mkrsrc(ws + WS_H2F, S_ * D_ * 2);
    float s1 = 0.f, s2 = 0.f;
#pragma unroll
    for (int j = 0; j < 2; ++j) {
      float y[8];
      unpack8(u32x4{yw[j][0][0], yw[j][0][1], yw[j][1][0], yw[j][1][1]}, y);
#pragma unroll
      for (int e = 0; e < 8; ++e) { const float d = fmaf(gh[8 * j + e], y[e], xv[8 * j + e]); xv[8 * j + e] = d; s1 += d; s2 = fmaf(d, d, s2); }
    }
    STAMP(2, 3);
    s1 = red8(s1); s2 = red8(s2);
    if (l < 8) lds_st<f32x2>(sb + LN_ST + 512 + (l * 8 + w) * 8, f32x2{s1, s2});
    __builtin_amdgcn_sched_barrier(0);
#pragma unroll
    for (int j = 0; j < 2; ++j) {
      const int cj = cj0 + j * 8;
      st128<SC1>(r1, fo + (cj >> 2) * 8192 + (cj & 3) * 256, u32x4{pk2(xv[8 * j] + mean, xv[8 * j + 1] + mean), pk2(xv[8 * j + 2] + mean, xv[8 * j + 3] + mean),
                                                                   pk2(xv[8 * j + 4] + mean, xv[8 * j + 5] + mean), pk2(xv[8 * j + 6] + mean, xv[8 * j + 7] + mean)});
    }
    lds_bar();
    {
      f32x4 p = lds_ld<f32x4>(sb + LN_ST + 512 + (l & 7) * 64);
#pragma unroll
      for (int i = 1; i < 4; ++i) p += lds_ld<f32x4>(sb + LN_ST + 512 + (l & 7) * 64 + i * 16);
      const float m2 = (p[0] + p[2]) * (1.f / 1024.f);
      const float rs = __builtin_amdgcn_rsqf(fmaxf(fmaf(-m2, m2, (p[1] + p[3]) * (1.f / 1024.f)), 0.f) + 1e-5f), nm = -m2 * rs;
#pragma unroll
      for (int j = 0; j < 2; ++j) {
        const int cj = cj0 + j * 8;
        float h[8];
#pragma unroll
        for (int e = 0; e < 8; ++e) h[e] = fmaf(fmaf(xv[8 * j + e], rs, nm), cv[8 * j + e], sv[8 * j + e]);
        st128<SC1>(r2, fo + (cj >> 2) * 8192 + (cj & 3) * 256, u32x4{pk2(h[0], h[1]), pk2(h[2], h[3]), pk2(h[4], h[5]), pk2(h[6], h[7])});
      }
    }
    STAMP(2, 4);
    wait_vm<0>();
    wg_bar();
    if (w == 0 && l < 4) flag_add(ws, FL_LN + pm * 4 + l);
    STAMP(2, 7);
  }

  if (w >= 4) {
    {
      STAMP(3, 0);
      flag_wait(ws, (FL_LN + pm * 4 + (x >> 1)) * 128, 16);
      STAMP(3, 3);
      L1.run1(0 STAMP_PASS);
      wg_bar();
      STAMP(3, 7);
    }
    {
      STAMP(4, 0);
      L2.init(w - 4, l, sb, ws + WS_HF, S_ * F_ * 2, (pm * 112 + kh * 56) * 8192, w2, F_ * D_ * 2, kh * 1792 * 2048 + pc * 128);
      L2.ws = ws; L2.rF = mkrsrc(ws + WS_FLG, FL_LINES * 128); L2.ldsF = sb + G2::LDS + G2::SB + (w - 4) * 256;
      L2.flag = (FL_G1 + (pm * 2 + kh) * 2 + ((x >> 1) & 1)) * 128;
      L2.own = pn & 7;
      L2.prefetchB();
      wg_bar();
      L2.run2(0 STAMP_PASS);
      STAMP(4, 7);
    }
    STAMP_FLUSH;
    return;
  }
  const int wm = w >> 1, wc = w & 1, g = l >> 4;
  u32x4 pv[4];
  rsrc_t rh = mkrsrc(ws + WS_HF, S_ * F_ * 2);
  const u32 hb = (u32)(((pm * 112 + pn * 7) * 8 + wm * 4) * 1024 + l * 16);
  const int fl = FL_G1 + (pm * 2 + kh) * 2 + l;
#define STORE4(P) _Pragma("unroll") for (int m = 0; m < 4; ++m) st128<SC1>(rh, hb + (wc * 4 + (P)) * 8192 + m * 1024, pv[m]);
  const u32 ha = sb + wc * 4 * G2::SB + wm * 4096 + l * 16;
#define KEEP4(P) _Pragma("unroll") for (int m = 0; m < 4; ++m) lds_st<u32x4>(ha + (P) * G2::SB + m * 1024, pv[m]);
  {
    STAMP(3, 0);
    Consumer<G1> c;
    c.init(wm, wc, l, sb, (pn & 1) * 2);
    wg_bar();
    STAMP(3, 1);
    c.template rdall<0>();
    c.template rounds<5>();
    c.template stepk<0, 0, true, true>();
    c.template stepk<1, 0, true, false>();
    c.done();
    STAMP(3, 2);
#define SILU4(P) _Pragma("unroll") for (int m = 0; m < 4; ++m) { const f32x4 a = c.acc[m][2 * (P)], d = c.acc[m][2 * (P) + 1];          \
      pv[m] = u32x4{silu_pk(a[0], a[1]), silu_pk(a[2], a[3]), silu_pk(d[0], d[1]), silu_pk(d[2], d[3])}; asm volatile("" : "+v"(pv[m])); }
    SILU4(0) STORE4(0) KEEP4(0)
    __builtin_amdgcn_sched_barrier(0);
    STAMP(3, 3);
    u32x2 own[2];
#pragma unroll
    for (int m = 0; m < 2; ++m) {
      const f32x4 a = wc ? c.acc[m][6] : c.acc[2 + m][6], o = wc ? c.acc[2 + m][6] : c.acc[m][6];
      lds_st<u32x2>(sb + XH_BASE + (w * 2 + m) * 512 + l * 8, u32x2{silu_pk(a[0], a[1]), silu_pk(a[2], a[3])});
      own[m] = u32x2{silu_pk(o[0], o[1]), silu_pk(o[2], o[3])}; asm volatile("" : "+v"(own[m]));
    }
    SILU4(1)
    lds_bar();
    STAMP(3, 4);
    wait_vm<0>();
    if (l < 2) flag_add(ws, fl);
    STAMP(3, 5);
#pragma unroll
    for (int m = 0; m < 2; ++m) {
      const u32x2 o = lds_ld<u32x2>(sb + XH_BASE + ((w ^ 1) * 2 + m) * 512 + l * 8);
      const u32 o0 = o[0], o1 = o[1], n0 = own[m][0], n1 = own[m][1];
      const u32x4 f = wc ? u32x4{o0, o1, n0, n1} : u32x4{n0, n1, o0, o1};
      st128<SC1>(rh, hb + 3 * 8192 + (wc * 2 + m) * 1024, f);
      lds_st<u32x4>(sb + 3 * G2::SB + (wm * 4 + wc * 2 + m) * 1024 + l * 16, f);
    }
    STORE4(1) KEEP4(1)
    __builtin_amdgcn_sched_barrier(0);
    SILU4(2)
    KEEP4(2)
#undef SILU4
    STAMP(3, 7);
  }
  {
    STAMP(4, 0);
    u32x2 gbp[2], xr[4][2];
    const int jt0 = (pc + wc * 4) * 4 + kh * 2;
    Consumer<G2> c;
    c.init(wm, wc, l, sb, kh ? 0 : 2);
    lds_bar();
    STAMP(4, 1);
    c.template rdall<0>();
    c.template steps<0, 4, false>();
    wait_vm<0>();
    if (l < 2) flag_add(ws, fl + 64);
    STORE4(2)
    c.spare();
    c.template steps<4, 7>();
    c.template stepk<7, 0, false, true, 8>();
    STAMP(4, 3);
    wait_vm<0>();
    if (l < 2) flag_add(ws, fl + 128);
    STAMP(4, 6);
    {
      rsrc_t rm = mkrsrc(ws + WS_MOD, 6144 * 2), r1 = mkrsrc(ws + WS_X1F, S_ * D_ * 2);
#pragma unroll
      for (int j = 0; j < 2; ++j) {
        gbp[j] = ld64<SC1>(rm, (u32)((5 * 1024 + (jt0 + j) * 16 + 4 * g) * 2));
#pragma unroll
        for (int m = 0; m < 4; ++m)
          xr[m][j] = ld64<SC1>(r1, (u32)(((pm * 32 + (jt0 >> 1)) * 8 + wm * 4 + m) * 1024 + ((2 * j + (g >> 1)) * 16 + (l & 15)) * 16 + (g & 1) * 8));
      }
    }
    c.template rounds<6>();
    c.done();
    STAMP(4, 2);
    rsrc_t rx = mkrsrc(ws + WS_XCH, 256 * 16384);
    f32x4 keep[4][2], oth[4][2];
#pragma unroll
    for (int m = 0; m < 4; ++m) {
      f32x4 away[2];
#pragma unroll
      for (int j = 0; j < 2; ++j) { keep[m][j] = c.acc[m][2 + j]; away[j] = c.acc[m][j]; }
      st128<SC1>(rx, (u32)(b * 16384 + w * 4096 + m * 1024 + l * 16), u32x4{pk2(away[0][0], away[0][1]), pk2(away[0][2], away[0][3]), pk2(away[1][0], away[1][1]), pk2(away[1][2], away[1][3])});
    }
    wait_vm<0>();
    if (l == 0) __hip_atomic_store((GAS u32*)(ws + WS_FLG + (size_t)(FL_XC + b) * 128 + w * 4), 1u, __ATOMIC_RELAXED, __HIP_MEMORY_SCOPE_AGENT);
    STAMP(4, 4);
    float gf[2][4];
#pragma unroll
    for (int j = 0; j < 2; ++j) {
      const u32 g0 = gbp[j][0], g1 = gbp[j][1];
      gf[j][0] = bflo(g0) * -0.69314718056f; gf[j][1] = bfhi(g0) * -0.69314718056f; gf[j][2] = bflo(g1) * -0.69314718056f; gf[j][3] = bfhi(g1) * -0.69314718056f;
#pragma unroll
      for (int m = 0; m < 4; ++m) {
        const u32 x0 = xr[m][j][0], x1 = xr[m][j][1];
        keep[m][j] = f32x4{fmaf(gf[j][0], keep[m][j][0], bflo(x0)), fmaf(gf[j][1], keep[m][j][1], bfhi(x0)), fmaf(gf[j][2], keep[m][j][2], bflo(x1)), fmaf(gf[j][3], keep[m][j][3], bfhi(x1))};
        asm volatile("" : "+v"(keep[m][j]));
      }
    }
    flag_wait(ws, (FL_XC + (b ^ 4)) * 128 + w * 4, 1);
    STAMP(4, 5);
#pragma unroll
    for (int m = 0; m < 4; ++m) {
      float f[8]; unpack8(ld128<SC1>(rx, (u32)((b ^ 4) * 16384 + w * 4096 + m * 1024 + l * 16)), f);
      oth[m][0] = f32x4{f[0], f[1], f[2], f[3]}; oth[m][1] = f32x4{f[4], f[5], f[6], f[7]};
    }
#pragma unroll
    for (int m = 0; m < 4; ++m) {
      u32x2 r[2];
#pragma unroll
      for (int j = 0; j < 2; ++j) {
        const f32x4 a = keep[m][j], o = oth[m][j];
        r[j] = u32x2{pk2(fmaf(gf[j][0], o[0], a[0]), fmaf(gf[j][1], o[1], a[1])), pk2(fmaf(gf[j][2], o[2], a[2]), fmaf(gf[j][3], o[3], a[3]))};
      }
      const u32 p0 = r[0][0], p1 = r[0][1], q0 = r[1][0], q1 = r[1][1];
      auto s0 = __builtin_amdgcn_permlane16_swap(p0, q0, false, false);
      auto s1 = __builtin_amdgcn_permlane16_swap(p1, q1, false, false);
      const u32 e0 = s0[0], f0 = s0[1], e1 = s1[0], f1 = s1[1];
      st128<SC1>(mkrsrc(out, S_ * D_ * 2), (u32)(((pm * 128 + wm * 64 + m * 16 + (l & 15)) * 1024 + (jt0 + (g & 1)) * 16 + 8 * (g >> 1)) * 2), u32x4{e0, e1, f0, f1});
    }
    STAMP(4, 7);
  }
  STAMP_END;
}

extern "C" void kernel_launch(void* const* d_in, const int* in_sizes, int n_in, void* d_out, int out_size, void* d_ws, size_t ws_size,
                              hipStream_t stream) {
  char* ws = (char*)d_ws;
  (void)hipFuncSetAttribute((const void*)k1_prep, hipFuncAttributeMaxDynamicSharedMemorySize, K1_LDS);
  (void)hipFuncSetAttribute((const void*)kp_main, hipFuncAttributeMaxDynamicSharedMemorySize, KP_LDS);
  k1_prep<<<256, 512, K1_LDS, stream>>>(d_in[1], d_in[2], d_in[3], d_in[4], d_in[9], d_in[10], ws);
  kp_main<<<256, 512, KP_LDS, stream>>>(d_in[0], d_in[4], d_in[5], d_in[7], d_in[8], ws, d_out);
}
```

```cpp
#include <hip/hip_runtime.h>
#include <hip/hip_bf16.h>

typedef unsigned u32;
typedef unsigned u32x2 __attribute__((ext_vector_type(2)));
typedef unsigned u32x4 __attribute__((ext_vector_type(4)));
typedef float f32x2 __attribute__((ext_vector_type(2)));
typedef float f32x4 __attribute__((ext_vector_type(4)));
typedef __bf16 bf16x2 __attribute__((ext_vector_type(2)));
typedef __bf16 bf16x4 __attribute__((ext_vector_type(4)));
typedef __bf16 bf16x8 __attribute__((ext_vector_type(8)));
#define LAS __attribute__((address_space(3)))
#define GAS __attribute__((address_space(1)))
#define DI __device__ __forceinline__
typedef __amdgpu_buffer_rsrc_t rsrc_t;

constexpr int S_ = 2048, D_ = 1024, F_ = 3584;
#define KP_CODE_BYTES 33920
constexpr int SC1 = 16;

constexpr size_t WS_KVT = 0;
constexpr size_t WS_YB = 4u << 20;
constexpr size_t WS_X1F = 8u << 20;
constexpr size_t WS_H2F = 12u << 20;
constexpr size_t WS_HF = 16u << 20;
constexpr size_t WS_XCH = 32u << 20;
constexpr size_t WS_MOD = 36u << 20;
constexpr size_t WS_FLG = WS_MOD + (64u << 10);
constexpr size_t WS_MODP = WS_MOD + (256u << 10);
constexpr int FL_A2A = 0, FL_LN = 64, FL_MOD = 128, FL_XC = 192, FL_G1 = 192 + 256, FL_LINES = FL_G1 + 3 * 64;

extern __shared__ __attribute__((aligned(16))) char dsm[];

DI rsrc_t mkrsrc(const void* p, u32 bytes) { return __builtin_amdgcn_make_buffer_rsrc((void*)p, 0, bytes, 0x00020000); }
DI float bflo(u32 w) { return __uint_as_float(w << 16); }
DI float bfhi(u32 w) { return __uint_as_float(w & 0xffff0000u); }
DI u32 pk2(float a, float b) { f32x2 v = {a, b}; bf16x2 r = __builtin_convertvector(v, bf16x2); return __builtin_bit_cast(u32, r); }
DI float silu_f(float x) { return x * __builtin_amdgcn_rcpf(1.f + __builtin_amdgcn_exp2f(x * -1.44269504089f)); }
DI u32 silu_pk(float a, float b) {
  f32x2 e = {__builtin_amdgcn_exp2f(a), __builtin_amdgcn_exp2f(b)};
  e = e + 1.f;
  return pk2(a * __builtin_amdgcn_rcpf(e[0]), b * __builtin_amdgcn_rcpf(e[1]));
}
template <int AUX> DI u32x4 ld128(rsrc_t r, u32 off) { return __builtin_amdgcn_raw_buffer_load_b128(r, off, 0, AUX); }
template <int AUX> DI u32x2 ld64(rsrc_t r, u32 off) { return __builtin_amdgcn_raw_buffer_load_b64(r, off, 0, AUX); }
template <int AUX> DI void st128(rsrc_t r, u32 off, u32x4 v) { __builtin_amdgcn_raw_buffer_store_b128(v, r, off, 0, AUX); }
template <int AUX> DI void st64(rsrc_t r, u32 off, u32x2 v) { __builtin_amdgcn_raw_buffer_store_b64(v, r, off, 0, AUX); }
DI bf16x4 tr_read(u32 a) { return __builtin_amdgcn_ds_read_tr16_b64_v4bf16((LAS bf16x4*)(size_t)a); }
DI bf16x8 cat8(bf16x4 a, bf16x4 b) { return __builtin_shufflevector(a, b, 0, 1, 2, 3, 4, 5, 6, 7); }
template <class T> DI auto lds_ld(u32 a) { return *(LAS T*)(size_t)a; }
template <class T, class V> DI void lds_st(u32 a, V v) { *(LAS T*)(size_t)a = v; }
DI void unpack8(u32x4 w, float* f) {
#pragma unroll
  for (int i = 0; i < 4; ++i) { u32 x = w[i]; f[2 * i] = bflo(x); f[2 * i + 1] = bfhi(x); }
}
template <int N> DI void wait_vm() { asm volatile("s_waitcnt vmcnt(%0)" ::"n"(N) : "memory"); }
DI void wg_bar() { asm volatile("s_barrier" ::: "memory"); }
DI void lds_bar() { asm volatile("s_waitcnt lgkmcnt(0)\n\ts_barrier" ::: "memory"); }
template <int CTRL> DI float dpp_add(float x) {
  return x + __int_as_float(__builtin_amdgcn_update_dpp(0, __float_as_int(x), CTRL, 0xf, 0xf, true));
}
DI float add_x16(float x) { const u32 u = __float_as_uint(x); auto r = __builtin_amdgcn_permlane16_swap(u, u, false, false); const u32 a = r[0], b = r[1]; return __uint_as_float(a) + __uint_as_float(b); }
DI float add_x32(float x) { const u32 u = __float_as_uint(x); auto r = __builtin_amdgcn_permlane32_swap(u, u, false, false); const u32 a = r[0], b = r[1]; return __uint_as_float(a) + __uint_as_float(b); }

DI void lds_meet(u32 addr, int l) {
  asm volatile("s_waitcnt lgkmcnt(0)" ::: "memory");
  LAS u32* cnt = (LAS u32*)(size_t)addr;
  if (l == 0) __hip_atomic_fetch_add(cnt, 1u, __ATOMIC_RELAXED, __HIP_MEMORY_SCOPE_WORKGROUP);
  while ((u32)__builtin_amdgcn_readfirstlane(__hip_atomic_load(cnt, __ATOMIC_RELAXED, __HIP_MEMORY_SCOPE_WORKGROUP)) < 4u) __builtin_amdgcn_s_sleep(1);
  asm volatile("" ::: "memory");
}
DI void flag_add(char* ws, int line) { __hip_atomic_fetch_add((GAS u32*)(ws + WS_FLG + (size_t)line * 128), 1u, __ATOMIC_RELAXED, __HIP_MEMORY_SCOPE_AGENT); }
DI void flag_wait(char* ws, u32 byteoff, u32 target) {
  const GAS u32* p = (const GAS u32*)(ws + WS_FLG + byteoff);
#pragma unroll 1
  for (u32 spins = 0; spins < (1u << 18); ++spins) {
    const u32 v = __hip_atomic_load(p, __ATOMIC_RELAXED, __HIP_MEMORY_SCOPE_AGENT);
    if (__all(v >= target)) break;
    __builtin_amdgcn_s_sleep(1);
  }
  __builtin_amdgcn_fence(__ATOMIC_ACQUIRE, "wavefront");
  asm volatile("" ::: "memory");
}
DI u32 touch(rsrc_t r, u32 voff) { return __builtin_amdgcn_raw_buffer_load_b32(r, voff, 0, 0); }

template <int AUX> DI void dma16(u32 voff, rsrc_t r, u32 soff, u32 ldsaddr) {
  if constexpr (AUX == SC1) asm volatile("s_mov_b32 m0, %2\n\ts_nop 0\n\tbuffer_load_dwordx4 %0, %1, %3 offen sc1 lds" ::"v"(voff), "s"(r), "s"(ldsaddr), "s"(soff) : "memory");
  else asm volatile("s_mov_b32 m0, %2\n\ts_nop 0\n\tbuffer_load_dwordx4 %0, %1, %3 offen lds" ::"v"(voff), "s"(r), "s"(ldsaddr), "s"(soff) : "memory");
}

DI void dma4(u32 voff, rsrc_t r, u32 soff, u32 ldsaddr) {
  asm volatile("s_mov_b32 m0, %2\n\ts_nop 0\n\tbuffer_load_dword %0, %1, %3 offen lds" ::"v"(voff), "s"(r), "s"(ldsaddr), "s"(soff) : "memory");
}
DI float red8(float x) { return add_x32(add_x16(dpp_add<0x128>(x))); }

constexpr int K1_GV = 64 * 320, K1_SC = K1_GV + 49152, K1_CNT = K1_SC + 256, K1_LDS = K1_CNT + 16 + 1024;
__global__ __launch_bounds__(512) void k1_prep(const void* __restrict__ k, const void* __restrict__ v, const void* __restrict__ shk,
                                               const void* __restrict__ ssmk, const void* __restrict__ cin, const void* __restrict__ wmod, char* __restrict__ ws) {
  const u32 sb = (u32)(size_t)(LAS char*)dsm;
  const int t = threadIdx.x, b = blockIdx.x;
  if (__builtin_amdgcn_readfirstlane(t) >= 256) {
    const int aw = __builtin_amdgcn_readfirstlane((t - 256) >> 6), l = t & 63, idx = b >> 3;
    const int r0 = (idx >> 2) * 128, col0 = (b & 7) * 768 + (idx & 3) * 192;
    rsrc_t rw = mkrsrc(wmod, 1024 * 6144 * 2);
    if (aw == 0) dma4((u32)(l * 4), mkrsrc(cin, 1024 * 2), (u32)(r0 * 2), sb + K1_SC);
    wg_bar();
#pragma unroll
    for (int i = 0; i < 3; ++i) {
      const int o = i * 1024 + l * 16, row = o / 384, cb = o % 384;
      const u32 vo = (u32)(row * 12288 + (cb ^ (((row >> 1) & 3) << 5)));
#pragma unroll
      for (int j = 0; j < 4; ++j) dma16<0>(vo, rw, (u32)((r0 + aw * 32 + j * 8) * 12288 + col0 * 2), sb + K1_GV + aw * 12288 + j * 3072 + i * 1024);
    }
    asm volatile("" ::: "memory");
#pragma unroll
    for (int i = 0; i < 4; ++i) dma16<0>((u32)(l * 16), mkrsrc(ssmk, D_ * S_ * 2), (u32)(((b & 7) * 32 + idx) * 16384 + aw * 4096 + i * 1024), sb + K1_CNT + 16);
    u32 lo, hi;
    asm volatile("s_getpc_b64 s[20:21]\n\ts_add_u32 s20, s20, kp_main@rel32@lo+4\n\ts_addc_u32 s21, s21, kp_main@rel32@hi+12\n\ts_mov_b32 %0, s20\n\ts_mov_b32 %1, s21"
                 : "=s"(lo), "=s"(hi) : : "s20", "s21", "scc");
    const u32 pc = touch(mkrsrc((const void*)(((unsigned long long)hi << 32) | lo), KP_CODE_BYTES), (u32)((idx * 16 + (t & 15)) * 128));
    wait_vm<5>();
    if (aw == 0) { const u32 cw = lds_ld<u32>(sb + K1_SC + l * 4); lds_st<u32>(sb + K1_SC + l * 4, pk2(silu_f(bflo(cw)), silu_f(bfhi(cw)))); }
    lds_meet(sb + K1_CNT + 4, l);
    const int g = l >> 4, row = 4 * g + ((l & 15) >> 2);
    f32x4 d[3] = {{0, 0, 0, 0}, {0, 0, 0, 0}, {0, 0, 0, 0}};
#pragma unroll
    for (int ks = 0; ks < 4; ++ks) {
      const bf16x8 sc = cat8(lds_ld<bf16x4>(sb + K1_SC + ks * 64 + g * 8), lds_ld<bf16x4>(sb + K1_SC + ks * 64 + 32 + g * 8));
#pragma unroll
      for (int j = 0; j < 3; ++j) {
        const u32 ad = sb + K1_GV + ks * 12288 + row * 384 + (((aw * 3 + j) ^ ((row >> 1) & 3)) << 5) + (l & 3) * 8;
        d[j] = __builtin_amdgcn_mfma_f32_16x16x32_bf16(sc, cat8(tr_read(ad), tr_read(ad + 16 * 384)), d[j], 0, 0, 0);
      }
    }
    if (g < 3) ((float*)(ws + WS_MODP))[(idx >> 2) * 6144 + col0 + aw * 48 + l] = g == 0 ? d[0][0] : g == 1 ? d[1][0] : d[2][0];
    wait_vm<0>();
    asm volatile("" ::"v"(pc));
    return;
  }
  const int dblk = b & 7, sblk = b >> 3;
  const int a = t >> 4, cc = t & 15;
  const int s0 = sblk * 64 + a * 4, ch0 = dblk * 128 + cc * 8;
  rsrc_t rk = mkrsrc(k, S_ * D_ * 2), rv = mkrsrc(v, S_ * D_ * 2), rs = mkrsrc(shk, D_ * 4 * 2);
  u32x4 kk[7], vv[4], sh[4];
#pragma unroll
  for (int j = 0; j < 7; ++j) kk[j] = ld128<0>(rk, (u32)((s0 - 3 + j) * 2048 + ch0 * 2));
#pragma unroll
  for (int j = 0; j < 4; ++j) vv[j] = ld128<0>(rv, (u32)((s0 + j) * 2048 + ch0 * 2));
#pragma unroll
  for (int j = 0; j < 4; ++j) sh[j] = ld128<0>(rs, (u32)(ch0 * 8 + j * 16));
  if (t == 0) lds_st<u32x2>(sb + K1_CNT, u32x2{0u, 0u});
  __builtin_amdgcn_sched_barrier(0);
  lds_bar();
  __builtin_amdgcn_sched_barrier(0);
  float shf[8][4];
#pragma unroll
  for (int j = 0; j < 4; ++j) { float f[8]; unpack8(sh[j], f);
#pragma unroll
    for (int i = 0; i < 8; ++i) shf[2 * j + (i >> 2)][i & 3] = f[i]; }
  float kf[7][8];
#pragma unroll
  for (int j = 0; j < 7; ++j) unpack8(kk[j], kf[j]);
#pragma unroll
  for (int r = 0; r < 4; ++r) {
    float vf[8], o[8]; unpack8(vv[r], vf);
#pragma unroll
    for (int e = 0; e < 8; ++e) {
      float x = kf[r][e] * shf[e][3];
      x = fmaf(kf[r + 1][e], shf[e][2], x);
      x = fmaf(kf[r + 2][e], shf[e][1], x);
      x = fmaf(kf[r + 3][e], shf[e][0], x);
      o[e] = x * vf[e];
    }
    u32x4 w = {pk2(o[0], o[1]), pk2(o[2], o[3]), pk2(o[4], o[5]), pk2(o[6], o[7])};
    lds_st<u32x4>(sb + (a * 4 + r) * 320 + cc * 16, w);
  }
  const int w = t >> 6, l = t & 63, g = l >> 4, q = (l & 15) >> 2, p = l & 3;
  lds_meet(sb + K1_CNT, l);
  rsrc_t rd = mkrsrc(ws + WS_KVT, S_ * D_ * 2);
#pragma unroll
  for (int i = 0; i < 4; ++i) {
    const int u = w * 4 + i, s8 = u >> 1, hf = u & 1;
    const u32 ad = sb + (s8 * 8 + q) * 320 + (hf * 64 + g * 16 + p * 4) * 2;
    bf16x8 r = cat8(tr_read(ad), tr_read(ad + 4 * 320));
    st128<0>(rd, (u32)((((sblk * 4 + (s8 >> 1)) * 256 + dblk * 32 + hf * 16 + (l >> 2)) * 8 + (s8 & 1) * 4 + (l & 3)) * 16), __builtin_bit_cast(u32x4, r));
  }
  if (t < 3 && b * 3 + t < FL_LINES) *(u32x4*)(ws + WS_FLG + (size_t)(b * 3 + t) * 128) = u32x4{0, 0, 0, 0};
}

template <int MODE_, int NT_, int KS_, int NS_, int LDB_, int PB_>
struct GC {
  static constexpr int MODE = MODE_, NT = NT_, KS = KS_, NS = NS_, SY = 2, LDB = LDB_;
  static constexpr int PB = PB_;
  static constexpr int OWN = MODE == 1 ? 0 : 7;
  static constexpr int NPB = MODE == 1 ? 4 : 2;
  static constexpr int PSTR = MODE == 1 ? 1088 : 1024, SB = 8192 + 4 * NPB * PSTR, NB = KS / SY, LDS = NS * SB;
  static constexpr int mn(int a, int b) { return a < b ? a : b; }
  static constexpr int need(int j) { return mn(SY * (j + 1), KS - 1); }
  static constexpr int issued(int j) { return j <= 0 ? NS - 1 : mn(SY * (j - 1) + NS, KS - 1); }
  static constexpr int cnt(int a, int b) { int n = 0; for (int s = a; s <= b; ++s) n += s < PB ? 2 : 2 + NPB; return n; }
  static constexpr int prod(int s) { return s < 21 ? (s - 7) >> 1 : s < 42 ? (s - 21) / 3 : (s - 42) >> 1; }
  static constexpr int kloc(int s) { return MODE == 1 || s < 7 ? s : s < 21 ? 4 * ((s - 7) & 1) : s < 42 ? 1 + 2 * ((s - 21) % 3) : 2 + 4 * (s & 1); }
  static constexpr int gate(int j) { return MODE == 1 || j < 0 ? 0 : (issued(j) < 21 && issued(j + 1) >= 21) ? 1 : (issued(j) < 42 && issued(j + 1) >= 42) ? 2 : 0; }
  static constexpr int slot(int s) { return MODE == 2 && s == NS ? NS : s % NS; }
  static constexpr int PD = (NS + SY) / SY - 2;
};

template <class C> struct Loader {
  u32 voffA, voffB; rsrc_t rA, rB, rF; u32 soffA, soffB, ldsA, ldsB, ldsF, flag; int own; char* ws;
  template <int ST> DI u32 kstep() {
    if constexpr (C::MODE == 1) return ST;
    else if constexpr (ST < C::OWN) return own * 7 + ST;
    else return (C::prod(ST) >= own ? 7 : 0) + C::prod(ST) * 7 + C::kloc(ST);
  }
  template <int ST> DI void stA() {
    const u32 so = soffA + kstep<ST>() * 8192;
#pragma unroll
    for (int i = 0; i < 2; ++i) dma16<SC1>(voffA, rA, so + i * 1024, ldsA + C::slot(ST) * C::SB + i * 1024);
  }
  template <int ST> DI void stB() {
    const u32 so = soffB + kstep<ST>() * (32 * C::LDB);
#pragma unroll
    for (int i = 0; i < C::NPB; ++i) {
      if constexpr (C::MODE == 1) dma16<0>(voffB, rB, so + 2 * i * C::LDB, ldsB + C::slot(ST) * C::SB + ((i & 1) + 4 * (i >> 1)) * C::PSTR);
      else dma16<0>(voffB ^ (i * 512), rB, so + 4 * i * C::LDB, ldsB + C::slot(ST) * C::SB + i * 1024);
    }
  }
  template <int A, int B, int WHAT> DI void stages() { if constexpr (A <= B) { if constexpr (WHAT & 1) stA<A>(); if constexpr (WHAT & 2) stB<A>(); stages<A + 1, B, WHAT>(); } }
  template <int J> DI void step() {
    if constexpr (J < C::NB) {
      wait_vm<C::cnt(C::need(J) + 1, C::issued(J)) + (C::MODE == 1 && C::issued(J) == C::KS - 1 ? 7 : 0)>();
      wg_bar();
      if constexpr (C::gate(J + C::PD) != 0) {
        static_assert(C::issued(J) + 1 <= C::need(J + C::PD), "the poll must be older than the stage the later wait covers");
        asm volatile("s_mov_b32 m0, %2\n\ts_nop 0\n\tbuffer_load_dword %0, %1, %3 offen sc1 lds" ::"v"(0u), "s"(rF), "s"(ldsF), "s"(flag + C::gate(J + C::PD) * 64 * 128) : "memory");
      }
      if constexpr (C::gate(J) != 0) {
        if ((u32)__builtin_amdgcn_readfirstlane(lds_ld<volatile u32>(ldsF)) < 32u) flag_wait(ws, flag + C::gate(J) * 64 * 128, 32);
      }
      stages<C::issued(J) + 1, C::issued(J + 1), 3>();
      if constexpr (C::MODE == 1 && C::issued(J) < C::KS - 1 && C::issued(J + 1) == C::KS - 1) {
#pragma unroll
        for (int i = 0; i < 7; ++i) dma16<0>(voffA, rF, flag + i * 1024, ldsF);
      }
      step<J + 1>();
    }
  }
  DI void init(int lw, int l, u32 sb, const void* Aptr, u32 Abytes, u32 aoff, const void* Bptr, u32 Bbytes, u32 boff) {
    rA = mkrsrc(Aptr, Abytes); rB = mkrsrc(Bptr, Bbytes);
    voffA = l * 16; soffA = aoff + lw * 2048; ldsA = sb + lw * 2048;
    soffB = boff + lw * 8 * C::LDB;
    if constexpr (C::MODE == 1) {
      const int h = l >> 5;
      voffB = h * C::LDB + (((l & 31) * 16) ^ (h * 32));
      ldsB = sb + 8192 + (2 * (lw & 1) + 8 * (lw >> 1)) * C::PSTR;
    } else {
      const int h = l >> 4;
      const int sg = ((l & 15) >> 1) ^ h;
      voffB = h * C::LDB + (sg >> 2) * 512 + (sg & 3) * 32 + (l & 1) * 16;
      ldsB = sb + 8192 + lw * 2048;
    }
  }
  DI void prefetchB() { stages<0, C::PB - 1, 2>(); }
  DI void run1() {
    stages<0, 1, 1>();
    wait_vm<2>();
    wg_bar();
    stages<2, C::PB - 1, 1>();
    stages<C::PB, C::NS - 1, 3>();
    step<0>();
  }
  DI void run2() {
    static_assert(C::PB == C::NS + 1 && C::OWN == C::NS - 1 && C::gate(2 + C::PD) == 0, "start-up");
    wait_vm<0>();
    wg_bar();
    flag_wait(ws, flag, 32);
    stages<C::OWN, C::NS, 1>();
    wg_bar();
    stages<C::NS + 1, C::issued(3), 3>();
    step<3>();
  }
};

template <class C> struct Consumer {
  static constexpr int NT = C::NT, NBASE = C::MODE == 1 ? 3 : NT, NR = 4 + 2 * NT, NM = 4 * NT, PRE = C::MODE == 1 ? 2 : 4;
  static constexpr int TOFF = C::MODE == 1 ? 4 * C::PSTR : 4096, WIN = C::NS / 2;
  static_assert(PRE + NR <= NM && C::NS % 2 == 0 && (WIN - 1) * C::SB + TOFF + 4096 < 65536, "slots");
  f32x4 acc[4][NT];
  u32x4 Af[2][4]; u32x2 Bl[2][NT], Bh[2][NT];
  u32 ac[3], bc[3][NBASE];
  template <int X, int SL, int R> DI void rd1() {
    constexpr int wi = SL / WIN, so = (SL % WIN) * C::SB;
    if constexpr (R >= 2 && R < 6) asm volatile("ds_read_b128 %0, %1 offset:%2" : "=v"(Af[X][R - 2]) : "v"(ac[wi]), "i"(so + (R - 2) * 1024) : "memory");
    else if constexpr (R < NR) {
      constexpr int q = R < 2 ? R : R - 4, j = q >> 1, h = q & 1, bi = C::MODE == 1 ? (j == 6 ? 2 : (j & 1)) : j, off = so + (C::MODE == 1 && j < 6 ? 32 * j : 0) + h * TOFF;
      if constexpr (h == 0) asm volatile("ds_read_b64_tr_b16 %0, %1 offset:%2" : "=v"(Bl[X][j]) : "v"(bc[wi][bi]), "i"(off) : "memory");
      else asm volatile("ds_read_b64_tr_b16 %0, %1 offset:%2" : "=v"(Bh[X][j]) : "v"(bc[wi][bi]), "i"(off) : "memory");
    }
  }
  template <int X, int K, bool ZERO = false> DI void mm1() {
    constexpr int j = K >> 2, m = (j & 1) ? 3 - (K & 3) : (K & 3);
    const u32x4 b = {Bl[X][j][0], Bl[X][j][1], Bh[X][j][0], Bh[X][j][1]};
    if constexpr (ZERO) asm volatile("v_mfma_f32_16x16x32_bf16 %0, %1, %2, 0" : "=&v"(acc[m][j]) : "v"(b), "v"(Af[X][m])); else
    asm volatile("v_mfma_f32_16x16x32_bf16 %0, %1, %2, %0" : "+v"(acc[m][j]) : "v"(b), "v"(Af[X][m]));
  }
  static constexpr int lastrd(int K) { int r = -1; for (int k = 0; k <= K; ++k) { const int j = k >> 2, m = (j & 1) ? 3 - (k & 3) : (k & 3), rb = j == 0 ? 1 : 2 * j + 5, ra = 2 + m; r = rb > r ? rb : r; r = ra > r ? ra : r; } return r; }
  template <int N> DI void wl() { __builtin_amdgcn_s_waitcnt(0xC07F | ((N > 15 ? 15 : N) << 8)); }
  template <int ST, int K, bool BAR, bool RD, int SL = (ST + 1) % C::NS, bool ZERO = false> DI void stepk() {
    if constexpr (K < NM) {
      if constexpr (K == 0 && PRE > 0) wl<NR - 1 - lastrd(PRE - 1)>();
      if constexpr (K == PRE) { wl<0>(); if constexpr (BAR) wg_bar(); }
      mm1<ST & 1, K, ZERO>();
      if constexpr (RD && K >= PRE) rd1<(ST & 1) ^ 1, SL, K - PRE>();
      stepk<ST, K + 1, BAR, RD, SL, ZERO>();
    }
  }
  template <int A, int B, bool BAR = true> DI void steps() { if constexpr (A < B) { stepk<A, 0, BAR && !(A & 1), true>(); steps<A + 1, B, BAR>(); } }
  template <int R> DI void rdall() { if constexpr (R < NR) { rd1<0, 0, R>(); rdall<R + 1>(); } }
  DI void init(int wm, int wc, int l, u32 sb, int coff) {
    const int g = l >> 4, q = (l & 15) >> 2, p = l & 3;
    ac[0] = sb + wm * 4096 + l * 16;
    if constexpr (C::MODE == 1) {
      const int base = sb + 8192 + ((q >> 1) + 2 * (g & 1) + 8 * (g >> 1)) * C::PSTR + (q & 1) * 512 + 8 * p + 32 * (8 * wc + coff);
      const int dl = (q & 1) * 32;
      bc[0][0] = base + dl;
      bc[0][1] = base - dl;
      bc[0][2] = wc ? base - 32 - dl : base + 192 + dl;
    } else {
      const int row = 4 * g + q;
#pragma unroll
      for (int j = 0; j < NT; ++j) bc[0][j] = sb + 8192 + row * 256 + 8 * p + 32 * ((NT * wc + (j ^ coff)) ^ (row & 7));
    }
    ac[1] = ac[0] + WIN * C::SB;
    asm volatile("" : "+v"(ac[0]), "+v"(ac[1]));
#pragma unroll
    for (int i = 0; i < NBASE; ++i) { bc[1][i] = bc[0][i] + WIN * C::SB; asm volatile("" : "+v"(bc[0][i]), "+v"(bc[1][i])); }
    if constexpr (C::MODE == 1) {
#pragma unroll
      for (int m = 0; m < 4; ++m)
#pragma unroll
        for (int j = 0; j < NT; ++j) { acc[m][j] = f32x4{0, 0, 0, 0}; asm volatile("" : "+v"(acc[m][j])); }
    }
  }
  DI void spare() {
    ac[2] = ac[0] + C::LDS; asm volatile("" : "+v"(ac[2]));
#pragma unroll
    for (int i = 0; i < NBASE; ++i) { bc[2][i] = bc[0][i] + C::LDS; asm volatile("" : "+v"(bc[2][i])); }
  }
  template <int N> DI void rounds() {
#pragma unroll 1
    for (int j = 0; j < N; ++j) steps<0, C::NS>();
  }
  DI void done() {
    asm volatile("s_waitcnt lgkmcnt(0)\n\ts_nop 7\n\ts_nop 1" ::: "memory");
#pragma unroll
    for (int m = 0; m < 4; ++m)
#pragma unroll
      for (int j = 0; j < NT; ++j) asm volatile("" : "+v"(acc[m][j]));
#pragma unroll
    for (int m = 0; m < 4; ++m) asm volatile("" ::"v"(Af[0][m]), "v"(Bl[0][m]), "v"(Bh[0][m]));
  }
};
typedef GC<1, 7, 32, 6, F_ * 2, 2> G1;
typedef GC<2, 4, 56, 8, D_ * 2, 9> G2;

constexpr int U_BASE = 128, U_STR = 4576, C_STR = 4160, C_CH = 4 * C_STR, C_BASE = U_BASE + 4 * U_STR, GP_BASE = C_BASE + 4 * C_CH + 256;
constexpr int LN_MOD = 4 * G1::SB, LN_MOD4 = 5 * G1::SB, LN_ST = LN_MOD4 + 2048;
static_assert(LN_MOD >= GP_BASE + 256 && G1::NS >= 6, "adaLN scratch");
constexpr int XH_BASE = G2::LDS + G2::SB + 1024;
constexpr int KP_LDS = G1::LDS + 1024;
static_assert(KP_LDS >= XH_BASE + 4096, "LDS");
static_assert(KP_LDS <= 163840, "LDS");

struct Conv {
  f32x4 acc[8]; u32x2 Al[2][8], Ah[2][8]; u32x4 B[2][8]; u32 aA, aB;
  static constexpr int seq(int k, bool tile) {
    int c = 0;
    for (int r = 0; r < 8; ++r) {
      const int n[5] = {7, r < 7 ? 6 : 0, r < 6 ? 5 : 1, r < 5 ? 4 : 2, 3}, i[5] = {r, r < 7 ? r : 0, r < 6 ? r : r - 6, r < 5 ? r : r - 5, r >> 1};
      for (int e = 0; e < ((r & 1) ? 4 : 5); ++e) { if (c == k) return tile ? n[e] : i[e]; ++c; }
    }
    return 0;
  }
  template <int X, int T0, int R> DI void rd1() {
    constexpr int i = R / 3, k = R % 3;
    if constexpr (k == 0) asm volatile("ds_read_b64_tr_b16 %0, %1 offset:%2" : "=v"(Al[X][i]) : "v"(aA), "i"(64 * (8 * i + T0) + 8) : "memory");
    if constexpr (k == 1) asm volatile("ds_read_b64_tr_b16 %0, %1 offset:%2" : "=v"(Ah[X][i]) : "v"(aA), "i"(64 * (8 * i + T0)) : "memory");
    if constexpr (k == 2) asm volatile("ds_read_b128 %0, %1 offset:%2" : "=v"(B[X][7 - i]) : "v"(aB), "i"(512 * (7 - i) + 64 * (2 - T0)) : "memory");
  }
  template <int X, int T0, int K> DI void stepk() {
    if constexpr (K < 36) {
      constexpr int n = seq(K, true), i = seq(K, false);
      const u32x4 a = {Al[X][i][0], Al[X][i][1], Ah[X][i][0], Ah[X][i][1]};
      asm volatile("v_mfma_f32_16x16x32_bf16 %0, %1, %2, %0" : "+v"(acc[n]) : "v"(a), "v"(B[X][n - i]));
      if constexpr (K < 24) rd1<X ^ 1, T0, K>();
      stepk<X, T0, K + 1>();
    }
  }
  template <int R> DI void rdall() { if constexpr (R < 24) { rd1<0, 0, R>(); rdall<R + 1>(); } }
};

DI void row_stats_a(const float (&x)[16], u32 slot, int w, int l) {
  float s = 0.f;
#pragma unroll
  for (int e = 0; e < 16; ++e) s += x[e];
  const float mw = red8(s) * (1.f / 128.f);
  float qv = 0.f;
#pragma unroll
  for (int e = 0; e < 16; ++e) { const float d = x[e] - mw; qv = fmaf(d, d, qv); }
  qv = red8(qv);
  if (l < 8) lds_st<f32x2>(slot + (l * 8 + w) * 8, f32x2{mw, qv});
}
DI void row_stats_b(u32 slot, int l, float& mean, float& rstd) {
  f32x4 p[4];
#pragma unroll
  for (int i = 0; i < 4; ++i) p[i] = lds_ld<f32x4>(slot + (l & 7) * 64 + i * 16);
  float m = 0.f;
#pragma unroll
  for (int i = 0; i < 4; ++i) m += p[i][0] + p[i][2];
  m *= 0.125f;
  float m2 = 0.f, dd = 0.f;
#pragma unroll
  for (int i = 0; i < 4; ++i) { m2 += p[i][1] + p[i][3]; const float d0 = p[i][0] - m, d1 = p[i][2] - m; dd = fmaf(d0, d0, dd); dd = fmaf(d1, d1, dd); }
  mean = m;
  rstd = __builtin_amdgcn_rsqf(fmaf(dd, 128.f, m2) * (1.f / 1024.f) + 1e-5f);
}

extern "C" __global__ __launch_bounds__(512, 2) void kp_main(const void* __restrict__ xin, const void* __restrict__ ssmk, const void* __restrict__ ssmd,
                                                             const void* __restrict__ w1, const void* __restrict__ w2, char* __restrict__ ws, void* __restrict__ out) {
  const u32 sb = (u32)(size_t)(LAS char*)dsm;
  const int t = threadIdx.x, b = blockIdx.x, w = __builtin_amdgcn_readfirstlane(t >> 6), l = t & 63;
  const int x = b & 7, idx = b >> 3, cg = x * 32 + idx;
  const int pm = (x & 1) * 8 + (idx & 7), pn = (x >> 1) * 4 + (idx >> 3), kh = x >> 2;
  const int pq = (pn & 7) ^ (pm & 3), pc = (pq & 3) + (pq >> 2) * 8;
  const int srow = pm * 128 + pn * 8 + (l & 7), cj0 = w * 16 + (l >> 3);
  u32x4 xw[2];
#define LOAD_X _Pragma("unroll") for (int j = 0; j < 2; ++j) xw[j] = ld128<0>(mkrsrc(xin, S_ * D_ * 2), (u32)(srow * 2048 + (cj0 + j * 8) * 16));
  float xv[16];
  Loader<G1> L1; Loader<G2> L2;

  {
    if (w < 4) {
      const int ch = cg * 4 + w;
      rsrc_t rk = mkrsrc(ssmk, D_ * S_ * 2);
      u32x4 kw[4]; u32x2 khl[4];
#pragma unroll
      for (int q = 0; q < 4; ++q) {
        const u32 o = (u32)(ch * 4096 + (l + 64 * q) * 16);
        kw[q] = ld128<0>(rk, o);
        khl[q] = ld64<0>(rk, o - 8);
      }
      u32x4 uw[4];
#pragma unroll
      for (int q = 0; q < 4; ++q) uw[q] = ld128<0>(mkrsrc(ws + WS_KVT, S_ * D_ * 2), (u32)((((t >> 3) + 32 * q) * 256 + cg) * 128 + (t & 7) * 16));
      const float dval = bflo(((const unsigned short*)ssmd)[ch]);
      if (t == 0) lds_st<u32x4>(sb + GP_BASE, u32x4{0u, 0u, 0u, 0u});
      __builtin_amdgcn_sched_barrier(0);
      lds_bar();
      __builtin_amdgcn_sched_barrier(0);
      const u32x4 z4 = {0, 0, 0, 0};
      if (l < 30) lds_st<u32x4>(sb + U_BASE + w * U_STR + l * 16, z4);
      if (l < 8) lds_st<u32x4>(sb + C_BASE + w * C_CH + (l >> 1) * C_STR + (l & 1) * 16, z4);
      if (l == 0) khl[0] = u32x2{0, 0};
#pragma unroll
      for (int q = 0; q < 4; ++q) {
        const u32 wm2 = khl[q][0], wm1 = khl[q][1], w0 = kw[q][0], w1_ = kw[q][1], w2_ = kw[q][2], w3 = kw[q][3];
        const u32 am = __builtin_amdgcn_alignbit(wm1, wm2, 16), a0 = __builtin_amdgcn_alignbit(w0, wm1, 16), a1 = __builtin_amdgcn_alignbit(w1_, w0, 16),
                  a2 = __builtin_amdgcn_alignbit(w2_, w1_, 16), a3 = __builtin_amdgcn_alignbit(w3, w2_, 16);
        const u32 ad = sb + C_BASE + w * C_CH + 32 + (l + 64 * q) * 16;
        lds_st<u32x4>(ad + 0 * C_STR, kw[q]);
        lds_st<u32x4>(ad + 1 * C_STR, u32x4{a0, a1, a2, a3});
        lds_st<u32x4>(ad + 2 * C_STR, u32x4{wm1, w0, w1_, w2_});
        lds_st<u32x4>(ad + 3 * C_STR, u32x4{am, a0, a1, a2});
      }
#pragma unroll
      for (int q = 0; q < 4; ++q) lds_st<u32x4>(sb + U_BASE + (t & 3) * U_STR + 480 + ((t >> 2) + 64 * q) * 16, uw[q]);
      LOAD_X
      asm volatile("" ::"v"(dval));
      lds_meet(sb + GP_BASE, l);
      const int r = l & 15, g = l >> 4;
      Conv cv;
      cv.aA = sb + C_BASE + w * C_CH + (r >> 2) * C_STR + 56 + 8 * (l & 3) - 16 * g;
      cv.aB = sb + U_BASE + w * U_STR + 32 * r + 16 * g + 320;
#pragma unroll
      for (int n = 0; n < 8; ++n) {
        const u32x2 u = lds_ld<u32x2>(sb + U_BASE + w * U_STR + 480 + 512 * n + 32 * r + 8 * g);
        const u32 u0 = u[0], u1 = u[1];
        cv.acc[n] = f32x4{bflo(u0), bfhi(u0), bflo(u1), bfhi(u1)} * dval;
        if (n == 0) cv.rdall<0>();
      }
#pragma unroll
      for (int n = 0; n < 8; ++n) asm volatile("" : "+v"(cv.acc[n]));
      asm volatile("s_nop 1");
#pragma unroll 1
      for (int it = 0; it < 4; ++it) {
        __builtin_amdgcn_s_waitcnt(0xC07F);
        cv.stepk<0, 1, 0>();
        __builtin_amdgcn_s_waitcnt(0xC07F);
        cv.stepk<1, 2, 0>();
        cv.aA += 128; cv.aB -= 128;
      }
      asm volatile("s_waitcnt lgkmcnt(0)\n\ts_nop 7\n\ts_nop 1" ::: "memory");
#pragma unroll
      for (int n = 0; n < 8; ++n) asm volatile("" : "+v"(cv.acc[n]) : "v"(cv.Al[0][n]), "v"(cv.Ah[0][n]), "v"(cv.B[0][n]));
      f32x4 (&acc)[8] = cv.acc;
      asm volatile("" : "+v"(xw[0]), "+v"(xw[1]));
      const u32 ay = sb + C_BASE + w * (C_CH + 64) + 16 * r + 4 * g;
#pragma unroll
      for (int n = 0; n < 8; ++n) {
        lds_st<u32>(ay + 256 * n, pk2(acc[n][0], acc[n][2]));
        lds_st<u32>(ay + 256 * n + 2048, pk2(acc[n][1], acc[n][3]));
      }
      lds_meet(sb + GP_BASE + 4, l);
      const int q = (l & 15) >> 2, p = l & 3;
      rsrc_t ry = mkrsrc(ws + WS_YB, S_ * D_ * 2);
#pragma unroll
      for (int i = 0; i < 4; ++i) {
        const int u = w * 4 + i;
        const u32 ad = sb + C_BASE + q * (C_CH + 64) + (u * 64 + g * 16 + p * 4) * 2;
        st128<SC1>(ry, (u32)(cg * 16384 + u * 1024 + l * 16), __builtin_bit_cast(u32x4, cat8(tr_read(ad), tr_read(ad + 2048))));
      }
    } else {
      const int aw = w - 4;
      if (aw == 0) {
        __builtin_amdgcn_s_setprio(2);
        const int n0 = cg * 24 + min(l & 7, 5) * 4, which = n0 >> 10;
        const u32x4 v = ld128<0>(mkrsrc(ws + WS_MODP, 8 * 6144 * 4), (u32)(((l >> 3) * 6144 + n0) * 4));
        wg_bar();
        __builtin_amdgcn_sched_barrier(0);
        const u32 v0 = v[0], v1 = v[1], v2 = v[2], v3 = v[3];
        u32 p0 = pk2(red8(__uint_as_float(v0)), red8(__uint_as_float(v1))), p1 = pk2(red8(__uint_as_float(v2)), red8(__uint_as_float(v3)));
        if (which == 1 || which == 4) { p0 = pk2(1.f + bflo(p0), 1.f + bfhi(p0)); p1 = pk2(1.f + bflo(p1), 1.f + bfhi(p1)); }
        rsrc_t rm = mkrsrc(ws + WS_MOD, 6144 * 2);
        if (l < 6) st64<SC1>(rm, (u32)(n0 * 2), u32x2{p0, p1});
        wait_vm<0>();
        if (l < 8) flag_add(ws, FL_MOD + l * 8 + x);
        flag_wait(ws, (FL_MOD + x * 8 + (l & 7)) * 128, 32);
#pragma unroll
        for (int i = 0; i < 10; ++i) dma16<SC1>((u32)(l * 16), rm, i * 1024, sb + (i < 8 ? LN_MOD + i * 1024 : LN_MOD4 + (i - 8) * 1024));
        LOAD_X
        lds_st<volatile u32>(sb + GP_BASE + 8, 1u);
        wait_vm<0>();
        asm volatile("" : "+v"(xw[0]), "+v"(xw[1]));
        __builtin_amdgcn_s_setprio(0);
      } else {
        wg_bar();
        while ((u32)__builtin_amdgcn_readfirstlane(lds_ld<volatile u32>(sb + GP_BASE + 8)) == 0u) __builtin_amdgcn_s_sleep(4);
        LOAD_X
        asm volatile("" : "+v"(xw[0]), "+v"(xw[1]));
#pragma unroll
        for (int j = 0; j < 10; ++j) dma16<0>((u32)(l * 16), mkrsrc(w1, D_ * F_ * 2), (u32)((b * 28 + min((aw - 1) * 10 + j, 27)) * 1024), sb + GP_BASE + 1024);
      }
    }
    __builtin_amdgcn_sched_barrier(0);
    unpack8(xw[0], xv); unpack8(xw[1], xv + 8); row_stats_a(xv, sb + LN_ST, w, l);
    if (w < 4) wait_vm<0>();
    lds_bar();
    if (w == 0 && l < 8) flag_add(ws, FL_A2A + l * 8 + x);
  }
  if (w >= 4) {
    L1.init(w - 4, l, sb, ws + WS_H2F, S_ * D_ * 2, pm * 32 * 8192, w1, D_ * F_ * 2, pn * 448 - (pn & 1) * 64);
    L1.prefetchB();
    L1.rF = mkrsrc(w2, D_ * F_ * 2); L1.ldsF = sb + G1::LDS;
    L1.flag = (u32)((b * 28 + (w - 4) * 7) * 1024);
  }

  {
    float mean, rstd;
    row_stats_b(sb + LN_ST, l, mean, rstd);
    float gh[16], sv[16], cv[16];
#pragma unroll
    for (int j = 0; j < 2; ++j) {
      float sa[8], ca[8], ga[8];
      const u32 ad = sb + LN_MOD + (cj0 + j * 8) * 16;
      unpack8(lds_ld<u32x4>(ad), sa); unpack8(lds_ld<u32x4>(ad + 2048), ca); unpack8(lds_ld<u32x4>(ad + 4096), ga);
      unpack8(lds_ld<u32x4>(ad + 6144), sv + 8 * j); unpack8(lds_ld<u32x4>(ad + (LN_MOD4 - LN_MOD)), cv + 8 * j);
#pragma unroll
      for (int e = 0; e < 8; ++e) {
        xv[8 * j + e] -= mean;
        gh[8 * j + e] = ga[e] * fmaf(xv[8 * j + e] * rstd, ca[e], sa[e]);
        sv[8 * j + e] *= -1.44269504089f; cv[8 * j + e] *= -1.44269504089f;
      }
    }
#pragma unroll
    for (int e = 0; e < 16; ++e) asm volatile("" : "+v"(gh[e]), "+v"(sv[e]), "+v"(cv[e]), "+v"(xv[e]));
    if (w == 0) flag_wait(ws, (FL_A2A + x * 8 + (l & 7)) * 128, 32);
    wg_bar();
    rsrc_t ry = mkrsrc(ws + WS_YB, S_ * D_ * 2);
    u32x2 yw[2][2];
#pragma unroll
    for (int j = 0; j < 2; ++j)
#pragma unroll
      for (int h = 0; h < 2; ++h) yw[j][h] = ld64<SC1>(ry, (u32)(((2 * (cj0 + j * 8) + h) * 2048 + srow) * 8));
    const u32 fo = (u32)(pm * 32 * 8192 + (pn >> 1) * 1024 + (srow & 15) * 16);
    rsrc_t r1 = mkrsrc(ws + WS_X1F, S_ * D_ * 2), r2 = mkrsrc(ws + WS_H2F, S_ * D_ * 2);
    float s1 = 0.f, s2 = 0.f;
#pragma unroll
    for (int j = 0; j < 2; ++j) {
      float y[8];
      unpack8(u32x4{yw[j][0][0], yw[j][0][1], yw[j][1][0], yw[j][1][1]}, y);
#pragma unroll
      for (int e = 0; e < 8; ++e) { const float d = fmaf(gh[8 * j + e], y[e], xv[8 * j + e]); xv[8 * j + e] = d; s1 += d; s2 = fmaf(d, d, s2); }
    }
    s1 = red8(s1); s2 = red8(s2);
    if (l < 8) lds_st<f32x2>(sb + LN_ST + 512 + (l * 8 + w) * 8, f32x2{s1, s2});
    __builtin_amdgcn_sched_barrier(0);
#pragma unroll
    for (int j = 0; j < 2; ++j) {
      const int cj = cj0 + j * 8;
      st128<SC1>(r1, fo + (cj >> 2) * 8192 + (cj & 3) * 256, u32x4{pk2(xv[8 * j] + mean, xv[8 * j + 1] + mean), pk2(xv[8 * j + 2] + mean, xv[8 * j + 3] + mean),
                                                                   pk2(xv[8 * j + 4] + mean, xv[8 * j + 5] + mean), pk2(xv[8 * j + 6] + mean, xv[8 * j + 7] + mean)});
    }
    lds_bar();
    {
      f32x4 p = lds_ld<f32x4>(sb + LN_ST + 512 + (l & 7) * 64);
#pragma unroll
      for (int i = 1; i < 4; ++i) p += lds_ld<f32x4>(sb + LN_ST + 512 + (l & 7) * 64 + i * 16);
      const float m2 = (p[0] + p[2]) * (1.f / 1024.f);
      const float rs = __builtin_amdgcn_rsqf(fmaxf(fmaf(-m2, m2, (p[1] + p[3]) * (1.f / 1024.f)), 0.f) + 1e-5f), nm = -m2 * rs;
#pragma unroll
      for (int j = 0; j < 2; ++j) {
        const int cj = cj0 + j * 8;
        float h[8];
#pragma unroll
        for (int e = 0; e < 8; ++e) h[e] = fmaf(fmaf(xv[8 * j + e], rs, nm), cv[8 * j + e], sv[8 * j + e]);
        st128<SC1>(r2, fo + (cj >> 2) * 8192 + (cj & 3) * 256, u32x4{pk2(h[0], h[1]), pk2(h[2], h[3]), pk2(h[4], h[5]), pk2(h[6], h[7])});
      }
    }
    wait_vm<0>();
    wg_bar();
    if (w == 0 && l < 4) flag_add(ws, FL_LN + pm * 4 + l);
  }

  if (w >= 4) {
    {
      flag_wait(ws, (FL_LN + pm * 4 + (x >> 1)) * 128, 16);
      L1.run1();
      wg_bar();
    }
    {
      L2.init(w - 4, l, sb, ws + WS_HF, S_ * F_ * 2, (pm * 112 + kh * 56) * 8192, w2, F_ * D_ * 2, kh * 1792 * 2048 + pc * 128);
      L2.ws = ws; L2.rF = mkrsrc(ws + WS_FLG, FL_LINES * 128); L2.ldsF = sb + G2::LDS + G2::SB + (w - 4) * 256;
      L2.flag = (FL_G1 + (pm * 2 + kh) * 2 + ((x >> 1) & 1)) * 128;
      L2.own = pn & 7;
      L2.prefetchB();
      wg_bar();
      L2.run2();
    }
    return;
  }
  const int wm = w >> 1, wc = w & 1, g = l >> 4;
  u32x4 pv[4];
  rsrc_t rh = mkrsrc(ws + WS_HF, S_ * F_ * 2);
  const u32 hb = (u32)(((pm * 112 + pn * 7) * 8 + wm * 4) * 1024 + l * 16);
  const int fl = FL_G1 + (pm * 2 + kh) * 2 + l;
#define STORE4(P) _Pragma("unroll") for (int m = 0; m < 4; ++m) st128<SC1>(rh, hb + (wc * 4 + (P)) * 8192 + m * 1024, pv[m]);
  const u32 ha = sb + wc * 4 * G2::SB + wm * 4096 + l * 16;
#define KEEP4(P) _Pragma("unroll") for (int m = 0; m < 4; ++m) lds_st<u32x4>(ha + (P) * G2::SB + m * 1024, pv[m]);
  {
    Consumer<G1> c;
    c.init(wm, wc, l, sb, (pn & 1) * 2);
    wg_bar();
    c.template rdall<0>();
    c.template rounds<5>();
    c.template stepk<0, 0, true, true>();
    c.template stepk<1, 0, true, false>();
    c.done();
#define SILU4(P) _Pragma("unroll") for (int m = 0; m < 4; ++m) { const f32x4 a = c.acc[m][2 * (P)], d = c.acc[m][2 * (P) + 1];          \
      pv[m] = u32x4{silu_pk(a[0], a[1]), silu_pk(a[2], a[3]), silu_pk(d[0], d[1]), silu_pk(d[2], d[3])}; asm volatile("" : "+v"(pv[m])); }
    SILU4(0) STORE4(0) KEEP4(0)
    __builtin_amdgcn_sched_barrier(0);
    u32x2 own[2];
#pragma unroll
    for (int m = 0; m < 2; ++m) {
      const f32x4 a = wc ? c.acc[m][6] : c.acc[2 + m][6], o = wc ? c.acc[2 + m][6] : c.acc[m][6];
      lds_st<u32x2>(sb + XH_BASE + (w * 2 + m) * 512 + l * 8, u32x2{silu_pk(a[0], a[1]), silu_pk(a[2], a[3])});
      own[m] = u32x2{silu_pk(o[0], o[1]), silu_pk(o[2], o[3])}; asm volatile("" : "+v"(own[m]));
    }
    SILU4(1)
    lds_bar();
    wait_vm<0>();
    if (l < 2) flag_add(ws, fl);
#pragma unroll
    for (int m = 0; m < 2; ++m) {
      const u32x2 o = lds_ld<u32x2>(sb + XH_BASE + ((w ^ 1) * 2 + m) * 512 + l * 8);
      const u32 o0 = o[0], o1 = o[1], n0 = own[m][0], n1 = own[m][1];
      const u32x4 f = wc ? u32x4{o0, o1, n0, n1} : u32x4{n0, n1, o0, o1};
      st128<SC1>(rh, hb + 3 * 8192 + (wc * 2 + m) * 1024, f);
      lds_st<u32x4>(sb + 3 * G2::SB + (wm * 4 + wc * 2 + m) * 1024 + l * 16, f);
    }
    STORE4(1) KEEP4(1)
    __builtin_amdgcn_sched_barrier(0);
    SILU4(2)
    KEEP4(2)
#undef SILU4
  }
  {
    u32x2 gbp[2], xr[4][2];
    const int jt0 = (pc + wc * 4) * 4 + kh * 2;
    Consumer<G2> c;
    c.init(wm, wc, l, sb, kh ? 0 : 2);
    lds_bar();
    c.template rdall<0>();
    c.template stepk<0, 0, false, true, 1, true>();
    c.template steps<1, 4, false>();
    wait_vm<0>();
    if (l < 2) flag_add(ws, fl + 64);
    STORE4(2)
    c.spare();
    c.template steps<4, 7>();
    c.template stepk<7, 0, false, true, 8>();
    wait_vm<0>();
    if (l < 2) flag_add(ws, fl + 128);
    {
      rsrc_t rm = mkrsrc(ws + WS_MOD, 6144 * 2), r1 = mkrsrc(ws + WS_X1F, S_ * D_ * 2);
#pragma unroll
      for (int j = 0; j < 2; ++j) {
        gbp[j] = ld64<SC1>(rm, (u32)((5 * 1024 + (jt0 + j) * 16 + 4 * g) * 2));
#pragma unroll
        for (int m = 0; m < 4; ++m)
          xr[m][j] = ld64<SC1>(r1, (u32)(((pm * 32 + (jt0 >> 1)) * 8 + wm * 4 + m) * 1024 + ((2 * j + (g >> 1)) * 16 + (l & 15)) * 16 + (g & 1) * 8));
      }
    }
    c.template rounds<6>();
    c.done();
    rsrc_t rx = mkrsrc(ws + WS_XCH, 256 * 16384);
    f32x4 keep[4][2], oth[4][2];
#pragma unroll
    for (int m = 0; m < 4; ++m) {
      f32x4 away[2];
#pragma unroll
      for (int j = 0; j < 2; ++j) { keep[m][j] = c.acc[m][2 + j]; away[j] = c.acc[m][j]; }
      st128<SC1>(rx, (u32)(b * 16384 + w * 4096 + m * 1024 + l * 16), u32x4{pk2(away[0][0], away[0][1]), pk2(away[0][2], away[0][3]), pk2(away[1][0], away[1][1]), pk2(away[1][2], away[1][3])});
    }
    wait_vm<0>();
    if (l == 0) __hip_atomic_store((GAS u32*)(ws + WS_FLG + (size_t)(FL_XC + b) * 128 + w * 4), 1u, __ATOMIC_RELAXED, __HIP_MEMORY_SCOPE_AGENT);
    float gf[2][4];
#pragma unroll
    for (int j = 0; j < 2; ++j) {
      const u32 g0 = gbp[j][0], g1 = gbp[j][1];
      gf[j][0] = bflo(g0) * -0.69314718056f; gf[j][1] = bfhi(g0) * -0.69314718056f; gf[j][2] = bflo(g1) * -0.69314718056f; gf[j][3] = bfhi(g1) * -0.69314718056f;
#pragma unroll
      for (int m = 0; m < 4; ++m) {
        const u32 x0 = xr[m][j][0], x1 = xr[m][j][1];
        keep[m][j] = f32x4{fmaf(gf[j][0], keep[m][j][0], bflo(x0)), fmaf(gf[j][1], keep[m][j][1], bfhi(x0)), fmaf(gf[j][2], keep[m][j][2], bflo(x1)), fmaf(gf[j][3], keep[m][j][3], bfhi(x1))};
        asm volatile("" : "+v"(keep[m][j]));
      }
    }
    flag_wait(ws, (FL_XC + (b ^ 4)) * 128 + w * 4, 1);
#pragma unroll
    for (int m = 0; m < 4; ++m) {
      float f[8]; unpack8(ld128<SC1>(rx, (u32)((b ^ 4) * 16384 + w * 4096 + m * 1024 + l * 16)), f);
      oth[m][0] = f32x4{f[0], f[1], f[2], f[3]}; oth[m][1] = f32x4{f[4], f[5], f[6], f[7]};
    }
#pragma unroll
    for (int m = 0; m < 4; ++m) {
      u32x2 r[2];
#pragma unroll
      for (int j = 0; j < 2; ++j) {
        const f32x4 a = keep[m][j], o = oth[m][j];
        r[j] = u32x2{pk2(fmaf(gf[j][0], o[0], a[0]), fmaf(gf[j][1], o[1], a[1])), pk2(fmaf(gf[j][2], o[2], a[2]), fmaf(gf[j][3], o[3], a[3]))};
      }
      const u32 p0 = r[0][0], p1 = r[0][1], q0 = r[1][0], q1 = r[1][1];
      auto s0 = __builtin_amdgcn_permlane16_swap(p0, q0, false, false);
      auto s1 = __builtin_amdgcn_permlane16_swap(p1, q1, false, false);
      const u32 e0 = s0[0], f0 = s0[1], e1 = s1[0], f1 = s1[1];
      st128<SC1>(mkrsrc(out, S_ * D_ * 2), (u32)(((pm * 128 + wm * 64 + m * 16 + (l & 15)) * 1024 + (jt0 + (g & 1)) * 16 + 8 * (g >> 1)) * 2), u32x4{e0, e1, f0, f1});
    }
  }
}

extern "C" void kernel_launch(void* const* d_in, const int* in_sizes, int n_in, void* d_out, int out_size, void* d_ws, size_t ws_size,
                              hipStream_t stream) {
  char* ws = (char*)d_ws;
  (void)hipFuncSetAttribute((const void*)k1_prep, hipFuncAttributeMaxDynamicSharedMemorySize, K1_LDS);
  (void)hipFuncSetAttribute((const void*)kp_main, hipFuncAttributeMaxDynamicSharedMemorySize, KP_LDS);
  k1_prep<<<256, 512, K1_LDS, stream>>>(d_in[1], d_in[2], d_in[3], d_in[4], d_in[9], d_in[10], ws);
  kp_main<<<256, 512, KP_LDS, stream>>>(d_in[0], d_in[4], d_in[5], d_in[7], d_in[8], ws, d_out);
}
```
